```python
import torch

def _softmax(x, axis=-1):
    return torch.softmax(x, dim=axis)

def _gelu(x):
    return torch.nn.functional.gelu(x, approximate='tanh')

def _silu(x):
    return torch.nn.functional.silu(x)

def _sigmoid(x):
    return torch.sigmoid(x)

def _layernorm(x, axis=-1, eps=1e-05):
    m = x.mean(dim=axis, keepdim=True)
    v = x.var(dim=axis, keepdim=True, unbiased=False)
    return (x - m) / torch.sqrt(v + eps)

def _scatter_add(tgt, idx, src, axis=0):
    return tgt.clone().index_add_(axis, idx, src)

def _segment_sum(src, seg, n):
    out = torch.zeros((n,) + src.shape[1:], dtype=src.dtype)
    return out.index_add_(0, seg, src)

def _pad(x, pw):
    flat = tuple((v for pair in reversed(pw) for v in pair))
    return torch.nn.functional.pad(x, flat)
kernel_name = 'parallel_sum__memory_aug_recurrent_mem_tr__dense_activation_relu__prompt'

def setup_inputs():

    _w = lambda a: (lambda t: t.to(torch.bfloat16) if t.is_floating_point() else t)(torch.as_tensor(a))

    def _A_setup():
        B, S, Dm = (1, 2048, 448)
        H, Dh = (16, 256)
        M, seg = (8, 32)
        seg = min(seg, S)
        S = S // seg * seg
        return {'x': _w((lambda *s: torch.randn(*s, dtype=torch.bfloat16) * 0.05)(B, S, Dm)), 'mem0': _w((lambda *s: torch.randn(*s, dtype=torch.bfloat16) * 1.6)(B, M, Dm)), 'Wq': _w((lambda *s: torch.randn(*s, dtype=torch.bfloat16) * 0.8)(Dm, H, Dh) * Dm ** (-0.5)), 'Wk': _w((lambda *s: torch.randn(*s, dtype=torch.bfloat16) * 0.8)(Dm, H, Dh) * Dm ** (-0.5)), 'Wv': _w((lambda *s: torch.randn(*s, dtype=torch.bfloat16) * 1.6)(Dm, H, Dh) * Dm ** (-0.5)), 'Wo': _w((lambda *s: torch.randn(*s, dtype=torch.bfloat16) * 1.6)(H, Dh, Dm) * (H * Dh) ** (-0.5)), 'seg_len': seg}
    _A_in = _A_setup()

    def _B_setup():
        B, S, D, Dff = (1, 2048, 448, 1792)
        return {'x': _w((lambda *s: torch.randn(*s, dtype=torch.bfloat16) * 0.05)(B, S, D)), 'w1': _w((lambda *s: torch.randn(*s, dtype=torch.bfloat16) * 0.05)(D, Dff)), 'w2': _w((lambda *s: torch.randn(*s, dtype=torch.bfloat16) * 0.05)(Dff, D))}
    _B_in = _B_setup()
    return {'A_x': _A_in['x'], 'A_mem0': _A_in['mem0'], 'A_Wq': _A_in['Wq'], 'A_Wk': _A_in['Wk'], 'A_Wv': _A_in['Wv'], 'A_Wo': _A_in['Wo'], 'A_seg_len': _A_in['seg_len'], 'B_x': _B_in['x'], 'B_w1': _B_in['w1'], 'B_w2': _B_in['w2']}

def reference(A_x, A_mem0, A_Wq, A_Wk, A_Wv, A_Wo, A_seg_len, B_x, B_w1, B_w2):

    def _A_ref(x, mem0, Wq, Wk, Wv, Wo, seg_len):
        B, S, Dm = x.shape
        H, Dh = (Wq.shape[1], Wq.shape[2])
        M = mem0.shape[1]
        scale = Dh ** (-0.5)
        n_seg = S // seg_len
        L = M + seg_len
        idx = torch.arange(L)
        mask = (idx[:, None] < M) | (idx[:, None] >= idx[None, :])
        mem = mem0
        outs = []
        for s in range(n_seg):
            xs = x[:, s * seg_len:(s + 1) * seg_len, :].clone()
            h = torch.concatenate([mem, xs], axis=1)
            q = torch.einsum('btd,dhe->bhte', h, Wq)
            k = torch.einsum('btd,dhe->bhte', h, Wk)
            v = torch.einsum('btd,dhe->bhte', h, Wv)
            scores = torch.einsum('bhqe,bhke->bhqk', q, k) * scale
            scores = torch.where(mask[None, None], scores, -1000000000.0)
            p = _softmax(scores, axis=-1)
            o = torch.einsum('bhqk,bhke->bhqe', p, v)
            o = torch.einsum('bhte,hed->btd', o, Wo)
            mem = o[:, :M, :].clone()
            outs.append(o[:, M:, :])
        return torch.concatenate(outs, axis=1)

    def _B_ref(x, w1, w2):
        h = torch.einsum('bsd,df->bsf', x, w1)
        zero = torch.zeros_like(h)
        h = torch.maximum(h, zero)
        return torch.einsum('bsf,fd->bsd', h, w2)
    x = A_x
    xn = _layernorm(x, axis=-1)
    return x + _A_ref(xn, A_mem0, A_Wq, A_Wk, A_Wv, A_Wo, A_seg_len) + _B_ref(xn, B_w1, B_w2)
```

```cpp
#include <hip/hip_runtime.h>
#include <hip/hip_bf16.h>
#include <stdint.h>

typedef unsigned short u16;
__device__ __forceinline__ float b2f(u16 v) { return __uint_as_float(((uint32_t)v) << 16); }
__device__ __forceinline__ u16 f2b(float f) {
    uint32_t u = __float_as_uint(f);
    u += 0x7FFFu + ((u >> 16) & 1u);
    return (u16)(u >> 16);
}
__device__ __forceinline__ float rb(float f) { return b2f(f2b(f)); }

#define S_ 2048
#define D_ 448
#define H_ 16
#define DH_ 256
#define M_ 8
#define SEG_ 32
#define NSEG_ 64
#define DFF_ 1792

__global__ void ln_kernel(const u16* x, u16* xn) {
    int row = blockIdx.x; int lane = threadIdx.x;
    float v[7]; float s = 0.f;
    for (int i = 0; i < 7; i++) { v[i] = b2f(x[row * D_ + lane + 64 * i]); s += v[i]; }
    for (int o = 32; o; o >>= 1) s += __shfl_xor(s, o);
    float mean = s * (1.0f / D_);
    float q = 0.f;
    for (int i = 0; i < 7; i++) { float d = v[i] - mean; q += d * d; }
    for (int o = 32; o; o >>= 1) q += __shfl_xor(q, o);
    float var = rb(q * (1.0f / D_));
    float mb = rb(mean);
    float den = rb(sqrtf(rb(var + 1e-5f)));
    for (int i = 0; i < 7; i++) xn[row * D_ + lane + 64 * i] = f2b(rb(v[i] - mb) / den);
}

__global__ void gemm_naive(const u16* A, const u16* B, u16* C, int I, int J, int K,
                           long sab, long sai, long sak, long sbb, long sbk, long sbj,
                           long scb, long sci, long scj, int relu) {
    long idx = (long)blockIdx.x * blockDim.x + threadIdx.x;
    int j = idx % J; int i = (idx / J) % I; int b = blockIdx.y;
    if (idx >= (long)I * J) return;
    const u16* a = A + b * sab + i * sai; const u16* bb = B + b * sbb + j * sbj;
    float acc = 0.f;
    for (int k = 0; k < K; k++) acc += b2f(a[k * sak]) * b2f(bb[k * sbk]);
    if (relu) acc = fmaxf(rb(acc), 0.f);
    C[b * scb + i * sci + j * scj] = f2b(acc);
}

__global__ __launch_bounds__(256) void attn_step(const u16* mem, const u16* xn, const u16* Ah, const u16* Bh, const u16* G, const u16* U,
                          float* Opart, int s) {
    __shared__ float T[M_][D_];
    __shared__ float Kp[M_][D_];
    __shared__ float Vm[M_][D_];
    __shared__ float hm[40][D_ + 1];
    __shared__ float P[40][40];
    int h = blockIdx.x, tid = threadIdx.x;
    for (int i = tid; i < 40 * D_; i += 256) {
        int r = i / D_, c = i % D_;
        hm[r][c] = r < M_ ? b2f(mem[r * D_ + c]) : b2f(xn[(s * SEG_ + r - M_) * D_ + c]);
    }
    __syncthreads();
    const u16* A = Ah + (long)h * D_ * D_; const u16* B = Bh + (long)h * D_ * D_;
    for (int i = tid; i < M_ * D_; i += 256) {
        int r = i / D_, c = i % D_;
        float t = 0, kp = 0, vm = 0;
        for (int k = 0; k < D_; k++) {
            t += hm[r][k] * b2f(A[k * D_ + c]);
            kp += b2f(A[c * D_ + k]) * hm[r][k];
            vm += hm[r][k] * b2f(B[k * D_ + c]);
        }
        T[r][c] = rb(t); Kp[r][c] = rb(kp); Vm[r][c] = rb(vm);
    }
    __syncthreads();
    const u16* Gs = G + ((long)s * H_ + h) * D_ * SEG_;
    const u16* Us = U + ((long)s * H_ + h) * SEG_ * D_;
    for (int i = tid; i < 40 * 40; i += 256) {
        int q = i / 40, k = i % 40;
        float acc = 0;
        if (k >= M_) { for (int d = 0; d < D_; d++) acc += hm[q][d] * b2f(Gs[d * SEG_ + (k - M_)]); }
        else if (q < M_) { for (int d = 0; d < D_; d++) acc += T[q][d] * hm[k][d]; }
        else { for (int d = 0; d < D_; d++) acc += hm[q][d] * Kp[k][d]; }
        acc *= 0.0625f;
        bool ok = (q < M_) || (q >= k);
        P[q][k] = ok ? acc : -1e9f;
    }
    __syncthreads();
    if (tid < 40) {
        float mx = -3e38f; for (int k = 0; k < 40; k++) mx = fmaxf(mx, P[tid][k]);
        float sum = 0; for (int k = 0; k < 40; k++) { float e = __expf(P[tid][k] - mx); P[tid][k] = e; sum += e; }
        float inv = 1.0f / sum;
        for (int k = 0; k < 40; k++) P[tid][k] = rb(P[tid][k] * inv);
    }
    __syncthreads();
    for (int i = tid; i < 40 * D_; i += 256) {
        int q = i / D_, c = i % D_;
        float acc = 0;
        for (int k = 0; k < M_; k++) acc += P[q][k] * Vm[k][c];
        for (int k = 0; k < SEG_; k++) acc += P[q][M_ + k] * b2f(Us[k * D_ + c]);
        Opart[((long)h * 40 + q) * D_ + c] = acc;
    }
}

__global__ void reduce_step(const float* Opart, u16* mem_next, u16* oa, int s) {
    int i = blockIdx.x * 256 + threadIdx.x;
    if (i >= 40 * D_) return;
    float acc = 0;
    for (int h = 0; h < H_; h++) acc += Opart[(long)h * 40 * D_ + i];
    int q = i / D_, c = i % D_;
    if (q < M_) mem_next[q * D_ + c] = f2b(acc);
    else oa[(s * SEG_ + q - M_) * D_ + c] = f2b(acc);
}

__global__ void final_add(const u16* x, const u16* oa, const u16* ob, u16* out) {
    int i = blockIdx.x * 256 + threadIdx.x;
    out[i] = f2b(rb(b2f(x[i]) + b2f(oa[i])) + b2f(ob[i]));
}

extern "C" void kernel_launch(void* const* d_in, const int* in_sizes, int n_in,
                              void* d_out, int out_size, void* d_ws, size_t ws_size,
                              hipStream_t stream) {
    const u16* x = (const u16*)d_in[0];
    const u16* mem0 = (const u16*)d_in[1];
    const u16* Wq = (const u16*)d_in[2];
    const u16* Wk = (const u16*)d_in[3];
    const u16* Wv = (const u16*)d_in[4];
    const u16* Wo = (const u16*)d_in[5];
    const u16* w1 = (const u16*)d_in[8];
    const u16* w2 = (const u16*)d_in[9];
    u16* out = (u16*)d_out;
    char* ws = (char*)d_ws; size_t off = 0;
    auto alloc = [&](size_t bytes) { void* p = ws + off; off += (bytes + 255) & ~(size_t)255; return p; };
    u16* xn = (u16*)alloc((size_t)S_ * D_ * 2);
    u16* Ah = (u16*)alloc((size_t)H_ * D_ * D_ * 2);
    u16* Bh = (u16*)alloc((size_t)H_ * D_ * D_ * 2);
    u16* G = (u16*)alloc((size_t)NSEG_ * H_ * D_ * SEG_ * 2);
    u16* U = (u16*)alloc((size_t)NSEG_ * H_ * D_ * SEG_ * 2);
    u16* hid = (u16*)alloc((size_t)S_ * DFF_ * 2);
    u16* ob = (u16*)alloc((size_t)S_ * D_ * 2);
    u16* oa = (u16*)alloc((size_t)S_ * D_ * 2);
    u16* memb = (u16*)alloc((size_t)2 * M_ * D_ * 2);
    float* Opart = (float*)alloc((size_t)H_ * 40 * D_ * 4);

    ln_kernel<<<S_, 64, 0, stream>>>(x, xn);
    gemm_naive<<<dim3(D_ * D_ / 256, H_), 256, 0, stream>>>(Wq, Wk, Ah, D_, D_, DH_, DH_, H_ * DH_, 1, DH_, 1, H_ * DH_, (long)D_ * D_, D_, 1, 0);
    gemm_naive<<<dim3(D_ * D_ / 256, H_), 256, 0, stream>>>(Wv, Wo, Bh, D_, D_, DH_, DH_, H_ * DH_, 1, (long)DH_ * D_, D_, 1, (long)D_ * D_, D_, 1, 0);
    for (int s = 0; s < NSEG_; s++) {
        gemm_naive<<<dim3(D_ * SEG_ / 256, H_), 256, 0, stream>>>(Ah, xn + (long)s * SEG_ * D_, G + (long)s * H_ * D_ * SEG_, D_, SEG_, D_,
            (long)D_ * D_, D_, 1, 0, 1, D_, (long)D_ * SEG_, SEG_, 1, 0);
        gemm_naive<<<dim3(D_ * SEG_ / 256, H_), 256, 0, stream>>>(xn + (long)s * SEG_ * D_, Bh, U + (long)s * H_ * D_ * SEG_, SEG_, D_, D_,
            0, D_, 1, (long)D_ * D_, D_, 1, (long)D_ * SEG_, D_, 1, 0);
    }
    gemm_naive<<<dim3(S_ * DFF_ / 256, 1), 256, 0, stream>>>(xn, w1, hid, S_, DFF_, D_, 0, D_, 1, 0, DFF_, 1, 0, DFF_, 1, 1);
    gemm_naive<<<dim3(S_ * D_ / 256, 1), 256, 0, stream>>>(hid, w2, ob, S_, D_, DFF_, 0, DFF_, 1, 0, D_, 1, 0, D_, 1, 0);
    for (int s = 0; s < NSEG_; s++) {
        const u16* mcur = s == 0 ? mem0 : memb + (size_t)(s & 1) * M_ * D_;
        u16* mnext = memb + (size_t)((s + 1) & 1) * M_ * D_;
        attn_step<<<H_, 256, 0, stream>>>(mcur, xn, Ah, Bh, G, U, Opart, s);
        reduce_step<<<(40 * D_ + 255) / 256, 256, 0, stream>>>(Opart, mnext, oa, s);
    }
    final_add<<<S_ * D_ / 256, 256, 0, stream>>>(x, oa, ob, out);
}
```

```cpp
#include <hip/hip_runtime.h>
#include <stdint.h>

typedef unsigned short u16;
typedef __bf16 bf16x2 __attribute__((ext_vector_type(2)));
typedef __bf16 bf16x8 __attribute__((ext_vector_type(8)));
typedef __bf16 bf16x16 __attribute__((ext_vector_type(16)));
typedef short s16x4 __attribute__((ext_vector_type(4)));
typedef float f32x2 __attribute__((ext_vector_type(2)));
typedef float f32x4 __attribute__((ext_vector_type(4)));
typedef unsigned u32x2 __attribute__((ext_vector_type(2)));
typedef unsigned u32x4 __attribute__((ext_vector_type(4)));
typedef unsigned u32x8 __attribute__((ext_vector_type(8)));
#define DI __device__ __forceinline__
#define LAS __attribute__((address_space(3)))
#define GAS __attribute__((address_space(1)))
typedef __amdgpu_buffer_rsrc_t rsrc_t;

#define S_ 2048
#define D_ 448
#define NSEG 64

constexpr int RING_OFF = 0;
constexpr int XN_OFF = 512 * 1024;
constexpr int XNP_OFF = XN_OFF + S_ * D_ * 2;
constexpr int AIMG_OFF = XNP_OFF + S_ * D_ * 2;
constexpr int BIMG_OFF = AIMG_OFF + 16 * 28 * 14 * 1024;
constexpr int UIMG_OFF = BIMG_OFF + 16 * 28 * 14 * 1024;
constexpr int OB_OFF = UIMG_OFF + NSEG * 16 * 28 * 1024;
constexpr int MH_OFF = OB_OFF + S_ * D_ * 2;
constexpr int VM_OFF = MH_OFF + NSEG * 8192;
constexpr int PX_OFF = VM_OFF + NSEG * 4 * 28 * 1024;
constexpr int PM_OFF = PX_OFF + NSEG * 16 * 2048;
constexpr int FLAG_OFF = PM_OFF + NSEG * 8 * 1024;
constexpr int WS_END = FLAG_OFF + 65536;
constexpr unsigned SENT = 0xFFFFFFFFu;
constexpr int AUX_SC1 = 16;

struct Params {
    const u16* x; const u16* mem0; const u16* Wq; const u16* Wk; const u16* Wv; const u16* Wo;
    const u16* w1; const u16* w2; u16* out; char* ws;
};

DI float b2f(unsigned v) { return __uint_as_float(v << 16); }
DI float blo(unsigned v) { return __uint_as_float(v << 16); }
DI float bhi(unsigned v) { return __uint_as_float(v & 0xFFFF0000u); }
DI unsigned cvtpk(float lo, float hi) {
    f32x2 v = {lo, hi};
    return __builtin_bit_cast(unsigned, __builtin_convertvector(v, bf16x2));
}
DI float rb(float f) { return blo(cvtpk(f, f)); }
DI f32x4 mfma32(u32x4 a, u32x4 b, f32x4 c) {
    return __builtin_amdgcn_mfma_f32_16x16x32_bf16(__builtin_bit_cast(bf16x8, a), __builtin_bit_cast(bf16x8, b), c, 0, 0, 0);
}
DI f32x4 mfma16(u32x2 a, u32x2 b, f32x4 c) {
    return __builtin_amdgcn_mfma_f32_16x16x16bf16_1k(__builtin_bit_cast(s16x4, a), __builtin_bit_cast(s16x4, b), c, 0, 0, 0);
}
DI f32x4 smf(u32x4 a, u32x8 b, f32x4 c, int idx) {
    return __builtin_amdgcn_smfmac_f32_16x16x64_bf16(__builtin_bit_cast(bf16x8, a), __builtin_bit_cast(bf16x16, b), c, idx, 0, 0);
}
DI u32x8 cat(u32x4 a, u32x4 b) { return __builtin_shufflevector(a, b, 0, 1, 2, 3, 4, 5, 6, 7); }
DI rsrc_t mkrsrc(const void* p, int bytes) { return __builtin_amdgcn_make_buffer_rsrc((void*)p, 0, bytes, 0x00020000); }
template <int AUX> DI u32x4 bl128(rsrc_t r, int vo, int so) { return __builtin_amdgcn_raw_buffer_load_b128(r, vo, so, AUX); }
template <int AUX> DI u32x2 bl64(rsrc_t r, int vo, int so) { return __builtin_amdgcn_raw_buffer_load_b64(r, vo, so, AUX); }
template <int AUX> DI void bs128(u32x4 v, rsrc_t r, int vo, int so) { __builtin_amdgcn_raw_buffer_store_b128(v, r, vo, so, AUX); }
template <int AUX> DI void bs64(u32x2 v, rsrc_t r, int vo, int so) { __builtin_amdgcn_raw_buffer_store_b64(v, r, vo, so, AUX); }
template <int AUX> DI void bs32(unsigned v, rsrc_t r, int vo, int so) { __builtin_amdgcn_raw_buffer_store_b32(v, r, vo, so, AUX); }
DI u32x4 ldg128(const void* p) { return *(const GAS u32x4*)p; }
DI u32x2 ldg64(const void* p) { return *(const GAS u32x2*)p; }
DI u32x4 lds128(const char* p) { return *(const LAS u32x4*)p; }
DI u32x2 lds64(const char* p) { return *(const LAS u32x2*)p; }
DI u32x2 ldstr(const char* p) {
    typedef short v4s __attribute__((ext_vector_type(4)));
    return __builtin_bit_cast(u32x2, __builtin_amdgcn_ds_read_tr16_b64_v4i16((LAS v4s*)p));
}
DI u32x4 cat2(u32x2 a, u32x2 b) { return __builtin_shufflevector(a, b, 0, 1, 2, 3); }
DI void bar() { asm volatile("s_waitcnt lgkmcnt(0)\n\ts_barrier" ::: "memory"); }
DI void bar_vm() { asm volatile("s_waitcnt vmcnt(0) lgkmcnt(0)\n\ts_barrier" ::: "memory"); }
DI void dma16(const void* g, const char* ldsbase) {
    unsigned m = (unsigned)(uintptr_t)(const LAS char*)ldsbase;
    asm volatile("s_mov_b32 m0, %0\n\ts_nop 0\n\tglobal_load_lds_dwordx4 %1, off" ::"s"(m), "v"(g) : "memory");
}
template <int CTRL> DI float dppf(float v) { return __builtin_bit_cast(float, __builtin_amdgcn_update_dpp(0, __builtin_bit_cast(int, v), CTRL, 0xf, 0xf, true)); }
template <int CTRL> DI unsigned dppu(unsigned v) { return (unsigned)__builtin_amdgcn_update_dpp(0, (int)v, CTRL, 0xf, 0xf, true); }
#define DPP_XOR1 0xB1
#define DPP_XOR2 0x4E
#define DPP_HMIRROR 0x141
#define DPP_ROR8 0x128

DI void ln_rows(const Params& p, int row, int lane, u32x4 v) {
    float f[8];
#pragma unroll
    for (int i = 0; i < 4; i++) { f[2 * i] = blo(v[i]); f[2 * i + 1] = bhi(v[i]); }
    const bool act = lane < 56;
    float s = 0.f;
#pragma unroll
    for (int i = 0; i < 8; i++) s += f[i];
    if (!act) s = 0.f;
#pragma unroll
    for (int o = 32; o; o >>= 1) s += __shfl_xor(s, o);
    const float mean = s * (1.0f / D_);
    float q = 0.f;
#pragma unroll
    for (int i = 0; i < 8; i++) { float d = f[i] - mean; q += d * d; }
    if (!act) q = 0.f;
#pragma unroll
    for (int o = 32; o; o >>= 1) q += __shfl_xor(q, o);
    const float var = rb(q * (1.0f / D_));
    const float mb = rb(mean);
    const float den = rb(sqrtf(rb(var + 1e-5f)));
    u32x4 o;
#pragma unroll
    for (int i = 0; i < 4; i++) o[i] = cvtpk(rb(f[2 * i] - mb) / den, rb(f[2 * i + 1] - mb) / den);
    const bool b = lane & 1;
    unsigned s0 = b ? o[0] : o[2], s1 = b ? o[1] : o[3];
    unsigned r0 = dppu<DPP_XOR1>(s0), r1 = dppu<DPP_XOR1>(s1);
    u32x4 q4 = b ? u32x4{r0, o[2], r1, o[3]} : u32x4{o[0], r0, o[1], r1};
    if (act) {
        *(GAS u32x4*)(p.ws + XN_OFF + row * 896 + lane * 16) = o;
        *(GAS u32x4*)(p.ws + XNP_OFF + row * 896 + lane * 16) = q4;
    }
}

__global__ __launch_bounds__(256, 1) void k_prep(Params p) {
    __shared__ __attribute__((aligned(16))) char smem[57344];
    const int b = blockIdx.x, tid = threadIdx.x, lane = tid & 63;
    const int w = __builtin_amdgcn_readfirstlane(tid >> 6);
    const int g = lane >> 4, r16 = lane & 15;
    const int row0 = b * 8 + w * 2;
    const int lc = lane < 56 ? lane : 0;
    u32x4 xr0 = ldg128(p.x + row0 * D_ + lc * 8), xr1 = ldg128(p.x + (row0 + 1) * D_ + lc * 8);
    const int drow = 16 * (r16 >> 3) + 4 * ((r16 >> 2) & 1) + (r16 & 1) + 8 * ((r16 >> 1) & 1);
    if (b < 112) {
        const int h = b / 7, bp = b % 7;
        f32x4 acc[2][2][7];
#pragma unroll
        for (int i = 0; i < 2; i++)
#pragma unroll
            for (int j = 0; j < 2; j++)
#pragma unroll
                for (int k = 0; k < 7; k++) acc[i][j][k] = f32x4{0, 0, 0, 0};
        const u16* qa = p.Wq + ((size_t)(64 * bp + drow) * 16 + h) * 256 + 8 * g;
        const u16* kb = p.Wk + ((size_t)(112 * w + r16) * 16 + h) * 256 + 8 * g;
#pragma unroll
        for (int ks = 0; ks < 8; ks++) {
            u32x4 a[2][2];
#pragma unroll
            for (int i = 0; i < 2; i++)
#pragma unroll
                for (int j = 0; j < 2; j++) a[i][j] = ldg128(qa + (size_t)(32 * i + 2 * j) * 4096 + 32 * ks);
#pragma unroll
            for (int ft = 0; ft < 7; ft++) {
                u32x4 bo = ldg128(kb + (size_t)(16 * ft) * 4096 + 32 * ks);
#pragma unroll
                for (int i = 0; i < 2; i++)
#pragma unroll
                    for (int j = 0; j < 2; j++) acc[i][j][ft] = mfma32(a[i][j], bo, acc[i][j][ft]);
            }
        }
        ln_rows(p, row0, lane, xr0); ln_rows(p, row0 + 1, lane, xr1);
#pragma unroll
        for (int i = 0; i < 2; i++)
#pragma unroll
            for (int ft = 0; ft < 7; ft++) {
                u32x4 o = {cvtpk(acc[i][0][ft][0], acc[i][0][ft][1]), cvtpk(acc[i][0][ft][2], acc[i][0][ft][3]),
                           cvtpk(acc[i][1][ft][0], acc[i][1][ft][1]), cvtpk(acc[i][1][ft][2], acc[i][1][ft][3])};
                *(GAS u32x4*)(p.ws + AIMG_OFF + ((h * 28 + 7 * w + ft) * 14 + 2 * bp + i) * 1024 + lane * 16) = o;
            }
    } else if (b < 240) {
        const int j0 = b - 112, h = j0 >> 3, q = (j0 >> 1) & 3, half = j0 & 1;
#pragma unroll
        for (int i = 0; i < 14; i++) {
            const int ci = (w * 14 + i) * 64 + lane, e = ci / 14, cc = ci % 14;
            dma16(p.Wo + ((size_t)h * 256 + e) * D_ + 112 * q + 8 * cc, smem + (w * 14 + i) * 1024);
        }
        const int bp = 4 * half + w;
        const bool act = bp < 7;
        const int bpc = act ? bp : 0;
        u32x4 a[8][2][2];
        const u16* va = p.Wv + ((size_t)(64 * bpc + drow) * 16 + h) * 256 + 8 * g;
#pragma unroll
        for (int ks = 0; ks < 8; ks++)
#pragma unroll
            for (int i = 0; i < 2; i++)
#pragma unroll
                for (int j = 0; j < 2; j++) a[ks][i][j] = ldg128(va + (size_t)(32 * i + 2 * j) * 4096 + 32 * ks);
        ln_rows(p, row0, lane, xr0); ln_rows(p, row0 + 1, lane, xr1);
        bar_vm();
        if (act) {
            f32x4 acc[2][2][7];
#pragma unroll
            for (int i = 0; i < 2; i++)
#pragma unroll
                for (int j = 0; j < 2; j++)
#pragma unroll
                    for (int k = 0; k < 7; k++) acc[i][j][k] = f32x4{0, 0, 0, 0};
            const char* tb = smem + (8 * g + (r16 >> 2)) * 224 + (r16 & 3) * 8;
#pragma unroll
            for (int ks = 0; ks < 8; ks++)
#pragma unroll
                for (int ct = 0; ct < 7; ct++) {
                    u32x4 bo = cat2(ldstr(tb + ks * 32 * 224 + ct * 32), ldstr(tb + (ks * 32 + 4) * 224 + ct * 32));
#pragma unroll
                    for (int i = 0; i < 2; i++)
#pragma unroll
                        for (int j = 0; j < 2; j++) acc[i][j][ct] = mfma32(a[ks][i][j], bo, acc[i][j][ct]);
                }
#pragma unroll
            for (int i = 0; i < 2; i++)
#pragma unroll
                for (int ct = 0; ct < 7; ct++) {
                    u32x4 o = {cvtpk(acc[i][0][ct][0], acc[i][0][ct][1]), cvtpk(acc[i][0][ct][2], acc[i][0][ct][3]),
                               cvtpk(acc[i][1][ct][0], acc[i][1][ct][1]), cvtpk(acc[i][1][ct][2], acc[i][1][ct][3])};
                    *(GAS u32x4*)(p.ws + BIMG_OFF + ((h * 28 + 7 * q + ct) * 14 + 2 * bp + i) * 1024 + lane * 16) = o;
                }
        }
    } else {
        ln_rows(p, row0, lane, xr0); ln_rows(p, row0 + 1, lane, xr1);
        const int t = (b - 240) * 256 + tid;
        for (int i = t; i < 9 * 2048; i += 4096) {
            u32x4 v = {SENT, SENT, SENT, SENT};
            const int within = i & 511;
            if (i < 2048 && within < 448) v = ldg128(p.mem0 + (within & 7) * D_ + (within >> 3) * 8);
            *(GAS u32x4*)(p.ws + RING_OFF + i * 16) = v;
        }
        if (t < 448) *(GAS u32x4*)(p.ws + MH_OFF + t * 16) = ldg128(p.mem0 + (t & 7) * D_ + (t >> 3) * 8);
        if (t < 1024) *(GAS u32x4*)(p.ws + FLAG_OFF + t * 16) = u32x4{0, 0, 0, 0};
    }
}

template <int AUX> DI void u_item(const Params& p, rsrc_t ws, int tg, int h, int half, int w, int lane) {
    const int g = lane >> 4, r16 = lane & 15;
    const int t0 = 256 * tg + 64 * w;
    u32x4 a[2][2][14];
    const char* xa = p.ws + XNP_OFF + (t0 + 8 * (r16 >> 2) + (r16 & 3)) * 896 + g * 16;
#pragma unroll
    for (int s = 0; s < 2; s++)
#pragma unroll
        for (int ab = 0; ab < 2; ab++)
#pragma unroll
            for (int blk = 0; blk < 14; blk++) a[s][ab][blk] = ldg128(xa + (32 * s + 4 * ab) * 896 + blk * 64);
    const char* bb = p.ws + BIMG_OFF + (h * 28 + 14 * half) * 14 * 1024 + lane * 16;
    const int seg = 8 * tg + 2 * w;
    for (int ct = 0; ct < 14; ct++) {
        f32x4 acc[2][2] = {{{0, 0, 0, 0}, {0, 0, 0, 0}}, {{0, 0, 0, 0}, {0, 0, 0, 0}}};
        u32x4 bo[14];
#pragma unroll
        for (int blk = 0; blk < 14; blk++) bo[blk] = ldg128(bb + (ct * 14 + blk) * 1024);
#pragma unroll
        for (int blk = 0; blk < 14; blk++)
#pragma unroll
            for (int s = 0; s < 2; s++)
#pragma unroll
                for (int ab = 0; ab < 2; ab++) acc[s][ab] = mfma32(a[s][ab][blk], bo[blk], acc[s][ab]);
#pragma unroll
        for (int s = 0; s < 2; s++) {
            u32x4 o = {cvtpk(acc[s][0][0], acc[s][0][1]), cvtpk(acc[s][0][2], acc[s][0][3]), cvtpk(acc[s][1][0], acc[s][1][1]), cvtpk(acc[s][1][2], acc[s][1][3])};
            bs128<AUX>(o, ws, lane * 16, UIMG_OFF + (((seg + s) * 16 + h) * 28 + 14 * half + ct) * 1024);
        }
    }
}

template <int AUX> DI void ffn_item(const Params& p, rsrc_t ws, int it, int w, int lane, char* smem) {
    const int g = lane >> 4, r16 = lane & 15;
    const int t0 = 32 * it;
    char* stg = smem + w * 28672;
    char* hid = smem + 4 * 28672;
    u32x4 bx[2][14];
#pragma unroll
    for (int tt = 0; tt < 2; tt++)
#pragma unroll
        for (int ks = 0; ks < 14; ks++) bx[tt][ks] = ldg128(p.ws + XN_OFF + (t0 + 16 * tt + r16) * 896 + ks * 64 + g * 16);
    f32x4 acc2[7][2];
#pragma unroll
    for (int i = 0; i < 7; i++) { acc2[i][0] = f32x4{0, 0, 0, 0}; acc2[i][1] = f32x4{0, 0, 0, 0}; }
    int rw[7], cc[7];
#pragma unroll
    for (int i = 0; i < 7; i++) { const int ci = i * 64 + lane; rw[i] = ci / 14; cc[i] = ci % 14; }
    const char* tb = stg + (8 * g + (r16 >> 2)) * 224 + (r16 & 3) * 8;
    auto issue = [&](int n) {
        const int c = n / 28, ph = (n % 28) / 14, ks = n % 14;
#pragma unroll
        for (int i = 0; i < 7; i++) {
            const u16* src = ph == 0 ? p.w1 + (size_t)(32 * ks + rw[i]) * 1792 + 448 * c + 112 * w + 8 * cc[i]
                                     : p.w2 + (size_t)(448 * c + 32 * ks + rw[i]) * D_ + 112 * w + 8 * cc[i];
            dma16(src, stg + (n & 3) * 7168 + i * 1024);
        }
    };
    asm volatile("s_waitcnt vmcnt(0)" ::: "memory");
    issue(0); issue(1); issue(2);
    for (int c = 0; c < 4; c++) {
        f32x4 acc1[7][2];
#pragma unroll
        for (int i = 0; i < 7; i++) { acc1[i][0] = f32x4{0, 0, 0, 0}; acc1[i][1] = f32x4{0, 0, 0, 0}; }
#pragma unroll
        for (int ks = 0; ks < 14; ks++) {
            const int n = c * 28 + ks;
            asm volatile("s_waitcnt lgkmcnt(0)" ::: "memory");
            issue(n + 3);
            asm volatile("s_waitcnt vmcnt(21)" ::: "memory");
#pragma unroll
            for (int nt = 0; nt < 7; nt++) {
                u32x4 ao = cat2(ldstr(tb + (n & 3) * 7168 + nt * 32), ldstr(tb + (n & 3) * 7168 + 4 * 224 + nt * 32));
                acc1[nt][0] = mfma32(ao, bx[0][ks], acc1[nt][0]);
                acc1[nt][1] = mfma32(ao, bx[1][ks], acc1[nt][1]);
            }
        }
        bar();
#pragma unroll
        for (int nt = 0; nt < 7; nt++)
#pragma unroll
            for (int tt = 0; tt < 2; tt++) {
                f32x4 v = acc1[nt][tt];
                u32x2 o = {cvtpk(fmaxf(v[0], 0.f), fmaxf(v[1], 0.f)), cvtpk(fmaxf(v[2], 0.f), fmaxf(v[3], 0.f))};
                *(LAS u32x2*)(hid + (16 * tt + r16) * 912 + (112 * w + 16 * nt + 4 * g) * 2) = o;
            }
        bar();
#pragma unroll
        for (int ks = 0; ks < 14; ks++) {
            const int n = c * 28 + 14 + ks;
            asm volatile("s_waitcnt lgkmcnt(0)" ::: "memory");
            if (n + 3 < 112) { issue(n + 3); asm volatile("s_waitcnt vmcnt(21)" ::: "memory"); }
            else asm volatile("s_waitcnt vmcnt(0)" ::: "memory");
            u32x4 h0 = lds128(hid + r16 * 912 + ks * 64 + g * 16), h1 = lds128(hid + (16 + r16) * 912 + ks * 64 + g * 16);
#pragma unroll
            for (int nt = 0; nt < 7; nt++) {
                u32x4 ao = cat2(ldstr(tb + (n & 3) * 7168 + nt * 32), ldstr(tb + (n & 3) * 7168 + 4 * 224 + nt * 32));
                acc2[nt][0] = mfma32(ao, h0, acc2[nt][0]);
                acc2[nt][1] = mfma32(ao, h1, acc2[nt][1]);
            }
        }
    }
#pragma unroll
    for (int nt = 0; nt < 7; nt++)
#pragma unroll
        for (int tt = 0; tt < 2; tt++) {
            f32x4 v = acc2[nt][tt];
            u32x2 o = {cvtpk(v[0], v[1]), cvtpk(v[2], v[3])};
            bs64<AUX>(o, ws, (t0 + 16 * tt + r16) * 896 + (112 * w + 16 * nt + 4 * g) * 2, OB_OFF);
        }
    bar();
}

template <int AUX> DI void xa_item(const Params& p, rsrc_t ws, int G, int h, int w, int lane) {
    const int g = lane >> 4, r16 = lane & 15;
    const int s = 4 * G + w, t0 = 32 * s;
    u32x4 bx[2][14];
#pragma unroll
    for (int tt = 0; tt < 2; tt++)
#pragma unroll
        for (int blk = 0; blk < 14; blk++) bx[tt][blk] = ldg128(p.ws + XNP_OFF + (t0 + 16 * tt + r16) * 896 + blk * 64 + g * 16);
    const char* ab = p.ws + AIMG_OFF + h * 28 * 14 * 1024 + lane * 16;
    u32x4 qb[14][2];
    f32x4 sx[2][2] = {{{0, 0, 0, 0}, {0, 0, 0, 0}}, {{0, 0, 0, 0}, {0, 0, 0, 0}}};
    const char* kx = p.ws + XN_OFF + (t0 + 8 * (r16 >> 2) + (r16 & 3)) * 896 + g * 8;
#pragma unroll
    for (int pp = 0; pp < 14; pp++) {
        f32x4 acc[2][2] = {{{0, 0, 0, 0}, {0, 0, 0, 0}}, {{0, 0, 0, 0}, {0, 0, 0, 0}}};
#pragma unroll
        for (int a = 0; a < 2; a++)
#pragma unroll
            for (int blk = 0; blk < 14; blk++) {
                u32x4 ao = ldg128(ab + ((2 * pp + a) * 14 + blk) * 1024);
                acc[a][0] = mfma32(ao, bx[0][blk], acc[a][0]);
                acc[a][1] = mfma32(ao, bx[1][blk], acc[a][1]);
            }
#pragma unroll
        for (int tt = 0; tt < 2; tt++)
            qb[pp][tt] = u32x4{cvtpk(acc[0][tt][0], acc[0][tt][1]), cvtpk(acc[0][tt][2], acc[0][tt][3]), cvtpk(acc[1][tt][0], acc[1][tt][1]), cvtpk(acc[1][tt][2], acc[1][tt][3])};
#pragma unroll
        for (int kt = 0; kt < 2; kt++) {
            u32x4 ka = cat2(ldg64(kx + kt * 4 * 896 + pp * 64), ldg64(kx + kt * 4 * 896 + pp * 64 + 32));
            sx[kt][0] = mfma32(ka, qb[pp][0], sx[kt][0]);
            sx[kt][1] = mfma32(ka, qb[pp][1], sx[kt][1]);
        }
        __builtin_amdgcn_sched_barrier(0);
    }
    f32x4 sm[2] = {{0, 0, 0, 0}, {0, 0, 0, 0}};
    const int mo = (g >> 1) * 128 + (r16 & 7) * 16 + (g & 1) * 8;
#pragma unroll
    for (int pp = 0; pp < 14; pp++) {
        u32x4 ka = cat2(bl64<AUX>(ws, mo + pp * 512, MH_OFF + s * 8192), bl64<AUX>(ws, mo + pp * 512 + 256, MH_OFF + s * 8192));
        sm[0] = mfma32(ka, qb[pp][0], sm[0]);
        sm[1] = mfma32(ka, qb[pp][1], sm[1]);
    }
    const float sc = 0.0625f * 1.4426950408889634f;
#pragma unroll
    for (int tt = 0; tt < 2; tt++) {
        const int tok = 16 * tt + r16;
        float vx[8], vm[4];
        float mx = -3.0e38f;
#pragma unroll
        for (int kt = 0; kt < 2; kt++)
#pragma unroll
            for (int r = 0; r < 4; r++) {
                const int key = 8 * g + 4 * kt + r;
                const bool ok = key <= tok;
                vx[4 * kt + r] = ok ? sx[kt][tt][r] : -3.0e38f;
                mx = fmaxf(mx, vx[4 * kt + r]);
            }
#pragma unroll
        for (int r = 0; r < 4; r++) { vm[r] = g < 2 ? sm[tt][r] : -3.0e38f; mx = fmaxf(mx, vm[r]); }
        mx = fmaxf(mx, __shfl_xor(mx, 16)); mx = fmaxf(mx, __shfl_xor(mx, 32));
        float sum = 0.f;
#pragma unroll
        for (int i = 0; i < 8; i++) { vx[i] = vx[i] > -1.0e38f ? __builtin_amdgcn_exp2f((vx[i] - mx) * sc) : 0.f; sum += vx[i]; }
#pragma unroll
        for (int r = 0; r < 4; r++) { vm[r] = g < 2 ? __builtin_amdgcn_exp2f((vm[r] - mx) * sc) : 0.f; sum += vm[r]; }
        sum += __shfl_xor(sum, 16); sum += __shfl_xor(sum, 32);
        const float inv = 1.0f / sum;
        u32x4 o = {cvtpk(vx[0] * inv, vx[1] * inv), cvtpk(vx[2] * inv, vx[3] * inv), cvtpk(vx[4] * inv, vx[5] * inv), cvtpk(vx[6] * inv, vx[7] * inv)};
        bs128<AUX>(o, ws, lane * 16, PX_OFF + ((s * 16 + h) * 2 + tt) * 1024);
        u32x2 om = {cvtpk(vm[0] * inv, vm[1] * inv), cvtpk(vm[2] * inv, vm[3] * inv)};
        if (g < 2) bs64<AUX>(om, ws, ((h & 3) * 16 + r16) * 16 + g * 8, PM_OFF + ((s * 4 + (h >> 2)) * 2 + tt) * 1024);
    }
}

template <int AUX> DI void xb_item(const Params& p, rsrc_t ws, int s, int q, int w, int lane, char* smem) {
    f32x4 acc[7][2];
#pragma unroll
    for (int i = 0; i < 7; i++) { acc[i][0] = f32x4{0, 0, 0, 0}; acc[i][1] = f32x4{0, 0, 0, 0}; }
#pragma unroll
    for (int hh = 0; hh < 4; hh++) {
        const int h = 4 * w + hh;
        u32x4 p0 = bl128<AUX>(ws, lane * 16, PX_OFF + ((s * 16 + h) * 2) * 1024), p1 = bl128<AUX>(ws, lane * 16, PX_OFF + ((s * 16 + h) * 2 + 1) * 1024);
#pragma unroll
        for (int ct = 0; ct < 7; ct++) {
            u32x4 u = bl128<AUX>(ws, lane * 16, UIMG_OFF + ((s * 16 + h) * 28 + 7 * q + ct) * 1024);
            acc[ct][0] = mfma32(u, p0, acc[ct][0]);
            acc[ct][1] = mfma32(u, p1, acc[ct][1]);
        }
    }
    {
        u32x4 p0 = bl128<AUX>(ws, lane * 16, PM_OFF + ((s * 4 + w) * 2) * 1024), p1 = bl128<AUX>(ws, lane * 16, PM_OFF + ((s * 4 + w) * 2 + 1) * 1024);
#pragma unroll
        for (int ct = 0; ct < 7; ct++) {
            u32x4 u = bl128<AUX>(ws, lane * 16, VM_OFF + ((s * 4 + w) * 28 + 7 * q + ct) * 1024);
            acc[ct][0] = mfma32(u, p0, acc[ct][0]);
            acc[ct][1] = mfma32(u, p1, acc[ct][1]);
        }
    }
#pragma unroll
    for (int ct = 0; ct < 7; ct++)
#pragma unroll
        for (int tt = 0; tt < 2; tt++) *(LAS f32x4*)(smem + ((w * 14 + ct * 2 + tt) * 64 + lane) * 16) = acc[ct][tt];
    bar();
    for (int i = w * 64 + lane; i < 896; i += 256) {
        f32x4 o = *(LAS f32x4*)(smem + i * 16);
#pragma unroll
        for (int ww = 1; ww < 4; ww++) o += *(LAS f32x4*)(smem + (ww * 896 + i) * 16);
        const int l = i & 63, tt = (i >> 6) & 1, ct = i >> 7;
        const int t = 32 * s + 16 * tt + (l & 15), c = 112 * q + 16 * ct + 4 * (l >> 4);
        u32x2 xv = ldg64(p.x + t * D_ + c);
        u32x2 ob = bl64<AUX>(ws, (t * D_ + c) * 2, OB_OFF);
        u32x2 a = {cvtpk(o[0], o[1]), cvtpk(o[2], o[3])};
        u32x2 r = {cvtpk(blo(xv[0]) + blo(a[0]), bhi(xv[0]) + bhi(a[0])), cvtpk(blo(xv[1]) + blo(a[1]), bhi(xv[1]) + bhi(a[1]))};
        u32x2 y = {cvtpk(blo(r[0]) + blo(ob[0]), bhi(r[0]) + bhi(ob[0])), cvtpk(blo(r[1]) + blo(ob[1]), bhi(r[1]) + bhi(ob[1]))};
        *(GAS u32x2*)(p.out + t * D_ + c) = y;
    }
    bar();
}

DI unsigned max4(u32x4 v) { return max(max(v[0], v[1]), max(v[2], v[3])); }
constexpr unsigned SPIN_MAX = 1u << 17;

DI int m_slot(int k, int sel) { return RING_OFF + k * 32768 + sel * 8192; }
DI int p_slot(int k, int grp, int sel) { return RING_OFF + (3 + 2 * k + (grp >> 1)) * 32768 + sel * 8192 + (grp & 1) * 2560; }

DI void score_cu(const Params& p, rsrc_t ws, int h, int w, int lane, char* smem, int sel) {
    char* mimg = smem;
    char* Tb = smem + 8192 + w * 3584;
    char* Sb = smem + 8192 + 4 * 3584;
    char* Xb = smem + 32768 + w * 7168;
    const int g = lane >> 4, r16 = lane & 15;
    int kbq[7];
    kbq[0] = w; kbq[1] = w < 3 ? w + 4 : 6;
    {
        int n = 2;
#pragma unroll
        for (int k = 0; k < 7; k++) {   }
        int c = 0;
#pragma unroll
        for (int q = 2; q < 7; q++) {
            if (c == kbq[0]) c++;
            if (c == kbq[1]) c++;
            kbq[q] = c; c++;
        }
        (void)n;
    }
    u32x8 W[7][7];
#pragma unroll
    for (int ft = 0; ft < 7; ft++)
#pragma unroll
        for (int q = 0; q < 7; q++) {
            const int so = AIMG_OFF + ((h * 28 + 7 * w + ft) * 14 + 2 * kbq[q]) * 1024;
            W[ft][q] = cat(bl128<0>(ws, lane * 16, so), bl128<0>(ws, lane * 16, so + 1024));
        }
    const int idx = (lane & 8) ? 0xEEEE : 0x4444;
    const int keyc = 8 * ((r16 >> 2) & 1) + 4 * ((r16 >> 1) & 1) + 2 * (r16 >> 3) + (r16 & 1);
    const char* xg0 = p.ws + XN_OFF + keyc * 896 + (112 * w + 8 * g) * 2;
    const char* xg6 = p.ws + XN_OFF + (16 * (lane >> 5) + keyc) * 896 + (112 * w + 96 + 8 * ((lane >> 4) & 1)) * 2;
    auto xdma = [&](int s) {
        const int o = s * 32 * 896;
#pragma unroll
        for (int kk = 0; kk < 3; kk++) {
            dma16(xg0 + o + kk * 64, Xb + (2 * kk) * 1024);
            dma16(xg0 + o + kk * 64 + 16 * 896, Xb + (2 * kk + 1) * 1024);
        }
        dma16(xg6 + o, Xb + 6 * 1024);
    };
    xdma(0);
    const int gq = h >> 2, hh = h & 3;
    for (int s = 0; s < NSEG - 1; s++) {
        const int ms = m_slot(s % 3, sel);
        u32x4 m0, m1;
        for (unsigned spin = 0; spin < SPIN_MAX; spin++) {
            asm volatile("" ::: "memory");
            m0 = bl128<AUX_SC1>(ws, lane * 16, ms + kbq[0] * 1024);
            m1 = bl128<AUX_SC1>(ws, lane * 16, ms + kbq[1] * 1024);
            if (__all(max(max4(m0), max4(m1)) != SENT)) break;
        }
        asm volatile("s_waitcnt vmcnt(0)" ::: "memory");
        *(LAS u32x4*)(mimg + kbq[0] * 1024 + lane * 16) = m0;
        *(LAS u32x4*)(mimg + kbq[1] * 1024 + lane * 16) = m1;
        f32x4 T[7];
#pragma unroll
        for (int ft = 0; ft < 7; ft++) T[ft] = smf(m0, W[ft][0], f32x4{0, 0, 0, 0}, idx);
        bar();
        u32x4 mo[5];
#pragma unroll
        for (int q = 2; q < 7; q++) mo[q - 2] = lds128(mimg + kbq[q] * 1024 + lane * 16);
#pragma unroll
        for (int ft = 0; ft < 7; ft++) T[ft] = smf(m1, W[ft][1], T[ft], idx);
#pragma unroll
        for (int q = 2; q < 7; q++)
#pragma unroll
            for (int ft = 0; ft < 7; ft++) T[ft] = smf(mo[q - 2], W[ft][q], T[ft], idx);
#pragma unroll
        for (int ft = 0; ft < 7; ft++)
            *(LAS u32x2*)(Tb + (16 * ft + r16) * 32 + g * 8) = u32x2{cvtpk(T[ft][0], T[ft][1]), cvtpk(T[ft][2], T[ft][3])};
        f32x4 S0 = {0, 0, 0, 0}, S1 = S0, S2 = S0;
        const char* tr = Tb + (8 * g + (r16 >> 2)) * 32 + (r16 & 3) * 8;
#pragma unroll
        for (int kk = 0; kk < 3; kk++) {
            u32x4 tb = cat2(ldstr(tr + kk * 1024), ldstr(tr + kk * 1024 + 128));
            u32x4 km = lds128(mimg + (14 * w + 4 * kk + g) * 128 + (r16 & 7) * 16);
            u32x4 k1 = lds128(Xb + (2 * kk) * 1024 + lane * 16), k2 = lds128(Xb + (2 * kk + 1) * 1024 + lane * 16);
            S0 = mfma32(km, tb, S0); S1 = mfma32(k1, tb, S1); S2 = mfma32(k2, tb, S2);
        }
        {
            u32x2 tb = ldstr(Tb + (96 + 4 * g + (r16 >> 2)) * 32 + (r16 & 3) * 8);
            u32x2 km = lds64(mimg + (14 * w + 12 + (g >> 1)) * 128 + (r16 & 7) * 16 + (g & 1) * 8);
            u32x2 k1 = lds64(Xb + 6144 + ((g >> 1) * 16 + r16) * 16 + (g & 1) * 8);
            u32x2 k2 = lds64(Xb + 6144 + (32 + (g >> 1) * 16 + r16) * 16 + (g & 1) * 8);
            S0 = mfma16(km, tb, S0); S1 = mfma16(k1, tb, S1); S2 = mfma16(k2, tb, S2);
        }
#pragma unroll
        for (int r = 0; r < 4; r++) { S0[r] += dppf<DPP_ROR8>(S0[r]); S1[r] += dppf<DPP_ROR8>(S1[r]); S2[r] += dppf<DPP_ROR8>(S2[r]); }
        if ((lane & 8) == 0) {
            char* sb = Sb + w * 1536 + (r16 & 7) * 64 + g * 16;
            *(LAS f32x4*)(sb) = S0; *(LAS f32x4*)(sb + 512) = S1; *(LAS f32x4*)(sb + 1024) = S2;
        }
        bar();
        if (w == 0) {
            const int i = lane >> 3, c = lane & 7;
            f32x4 xs = *(LAS f32x4*)(Sb + 512 + (c >> 2) * 512 + i * 64 + (c & 3) * 16);
            float ms_ = *(LAS float*)(Sb + i * 64 + c * 4);
#pragma unroll
            for (int ww = 1; ww < 4; ww++) {
                xs += *(LAS f32x4*)(Sb + ww * 1536 + 512 + (c >> 2) * 512 + i * 64 + (c & 3) * 16);
                ms_ += *(LAS float*)(Sb + ww * 1536 + i * 64 + c * 4);
            }
            float mx = fmaxf(fmaxf(fmaxf(xs[0], xs[1]), fmaxf(xs[2], xs[3])), ms_);
            mx = fmaxf(mx, dppf<DPP_XOR1>(mx)); mx = fmaxf(mx, dppf<DPP_XOR2>(mx)); mx = fmaxf(mx, dppf<DPP_HMIRROR>(mx));
            const float sc = 0.0625f * 1.4426950408889634f;
            float e0 = __builtin_amdgcn_exp2f((xs[0] - mx) * sc), e1 = __builtin_amdgcn_exp2f((xs[1] - mx) * sc);
            float e2 = __builtin_amdgcn_exp2f((xs[2] - mx) * sc), e3 = __builtin_amdgcn_exp2f((xs[3] - mx) * sc);
            float em = __builtin_amdgcn_exp2f((ms_ - mx) * sc);
            float sum = (e0 + e1) + (e2 + e3) + em;
            sum += dppf<DPP_XOR1>(sum); sum += dppf<DPP_XOR2>(sum); sum += dppf<DPP_HMIRROR>(sum);
            const float inv = __builtin_amdgcn_rcpf(sum);
            u32x2 ox = {cvtpk(e0 * inv, e1 * inv), cvtpk(e2 * inv, e3 * inv)};
            float pm = em * inv;
            unsigned om = cvtpk(pm, dppf<DPP_XOR1>(pm));
            const int ps = p_slot(s % 3, gq, sel), pn = p_slot((s + 2) % 3, gq, sel);
            const int ox_off = 512 * (1 + hh) + 128 * (c >> 1) + 16 * i + 8 * (c & 1), om_off = 128 * hh + 16 * i + 2 * c;
            bs64<AUX_SC1>(ox, ws, ox_off, ps);
            if ((c & 1) == 0) bs32<AUX_SC1>(om, ws, om_off, ps);
            bs64<AUX_SC1>(u32x2{SENT, SENT}, ws, ox_off, pn);
            if ((c & 1) == 0) bs32<AUX_SC1>(SENT, ws, om_off, pn);
        }
        if (s + 1 < NSEG - 1) xdma(s + 1);
    }
}

DI void value_cu(const Params& p, rsrc_t ws, int v, int w, int lane, char* smem, int sel) {
    char* mimg = smem;
    char* red = smem + 8192;
    char* Wl = smem + 16384 + w * 16384;
    const int g = lane >> 4, r16 = lane & 15;
    int kbq[7];
    kbq[0] = w; kbq[1] = w < 3 ? w + 4 : 6;
    {
        int c = 0;
#pragma unroll
        for (int q = 2; q < 7; q++) {
            if (c == kbq[0]) c++;
            if (c == kbq[1]) c++;
            kbq[q] = c; c++;
        }
    }
    u32x8 W[4][2][6];
#pragma unroll
    for (int hh = 0; hh < 4; hh++)
#pragma unroll
        for (int ct = 0; ct < 2; ct++) {
#pragma unroll
            for (int q = 0; q < 6; q++) {
                const int so = BIMG_OFF + (((4 * w + hh) * 28 + 2 * v + ct) * 14 + 2 * kbq[q]) * 1024;
                W[hh][ct][q] = cat(bl128<0>(ws, lane * 16, so), bl128<0>(ws, lane * 16, so + 1024));
            }
            const int so = BIMG_OFF + (((4 * w + hh) * 28 + 2 * v + ct) * 14 + 2 * kbq[6]) * 1024;
            *(LAS u32x4*)(Wl + (hh * 2 + ct) * 2048 + lane * 16) = bl128<0>(ws, lane * 16, so);
            *(LAS u32x4*)(Wl + (hh * 2 + ct) * 2048 + 1024 + lane * 16) = bl128<0>(ws, lane * 16, so + 1024);
        }
    const int idx = (lane & 8) ? 0xEEEE : 0x4444;
    const int mi = lane >> 3, mc = lane & 7;
    const int moff = (4 * v + w) * 128 + mi * 16 + mc * 2;
    for (int s = 0; s < NSEG; s++) {
        const int ms = m_slot(s % 3, sel);
        u32x4 m0, m1;
        for (unsigned spin = 0; spin < SPIN_MAX; spin++) {
            asm volatile("" ::: "memory");
            m0 = bl128<AUX_SC1>(ws, lane * 16, ms + kbq[0] * 1024);
            m1 = bl128<AUX_SC1>(ws, lane * 16, ms + kbq[1] * 1024);
            if (__all(max(max4(m0), max4(m1)) != SENT)) break;
        }
        *(LAS u32x4*)(mimg + kbq[0] * 1024 + lane * 16) = m0;
        *(LAS u32x4*)(mimg + kbq[1] * 1024 + lane * 16) = m1;
        f32x4 V[4][2];
#pragma unroll
        for (int hh = 0; hh < 4; hh++)
#pragma unroll
            for (int ct = 0; ct < 2; ct++) V[hh][ct] = smf(m0, W[hh][ct][0], f32x4{0, 0, 0, 0}, idx);
        bar();
        if ((mc & 1) == 0) bs32<AUX_SC1>(SENT, ws, moff, m_slot((s + 2) % 3, sel));
        u32x4 mo[5];
#pragma unroll
        for (int q = 2; q < 7; q++) mo[q - 2] = lds128(mimg + kbq[q] * 1024 + lane * 16);
#pragma unroll
        for (int hh = 0; hh < 4; hh++)
#pragma unroll
            for (int ct = 0; ct < 2; ct++) V[hh][ct] = smf(m1, W[hh][ct][1], V[hh][ct], idx);
#pragma unroll
        for (int q = 2; q < 6; q++)
#pragma unroll
            for (int hh = 0; hh < 4; hh++)
#pragma unroll
                for (int ct = 0; ct < 2; ct++) V[hh][ct] = smf(mo[q - 2], W[hh][ct][q], V[hh][ct], idx);
#pragma unroll
        for (int hh = 0; hh < 4; hh++)
#pragma unroll
            for (int ct = 0; ct < 2; ct++) {
                u32x8 wl = cat(lds128(Wl + (hh * 2 + ct) * 2048 + lane * 16), lds128(Wl + (hh * 2 + ct) * 2048 + 1024 + lane * 16));
                V[hh][ct] = smf(mo[4], wl, V[hh][ct], idx);
            }
        u32x4 vb[2];
#pragma unroll
        for (int ct = 0; ct < 2; ct++) {
            float a[2][4];
#pragma unroll
            for (int b = 0; b < 2; b++)
#pragma unroll
                for (int r = 0; r < 4; r++) {
                    auto sw = __builtin_amdgcn_permlane32_swap(__float_as_uint(V[b][ct][r]), __float_as_uint(V[b + 2][ct][r]), false, false);
                    a[b][r] = __uint_as_float(sw[0]) + __uint_as_float(sw[1]);
                }
            vb[ct] = u32x4{cvtpk(a[0][0], a[0][1]), cvtpk(a[1][0], a[1][1]), cvtpk(a[0][2], a[0][3]), cvtpk(a[1][2], a[1][3])};
            const int so = VM_OFF + ((s * 4 + w) * 28 + 2 * v + ct) * 1024;
            const int vo = ((2 * (g >> 1)) * 16 + r16) * 16 + (g & 1) * 8;
            bs64<AUX_SC1>(u32x2{vb[ct][0], vb[ct][2]}, ws, vo, so);
            bs64<AUX_SC1>(u32x2{vb[ct][1], vb[ct][3]}, ws, vo + 256, so);
        }
        if (s == NSEG - 1) break;
        u32x4 U[4][2];
#pragma unroll
        for (int hh = 0; hh < 4; hh++)
#pragma unroll
            for (int ct = 0; ct < 2; ct++) U[hh][ct] = bl128<AUX_SC1>(ws, lane * 16, UIMG_OFF + ((s * 16 + 4 * w + hh) * 28 + 2 * v + ct) * 1024);
        const int ps = p_slot(s % 3, w, sel);
        u32x4 p0, p1, p2;
        for (unsigned spin = 0; spin < SPIN_MAX; spin++) {
            asm volatile("" ::: "memory");
            p0 = bl128<AUX_SC1>(ws, lane * 16, ps);
            p1 = bl128<AUX_SC1>(ws, lane * 16, ps + 1024);
            p2 = bl128<AUX_SC1>(ws, (lane & 31) * 16, ps + 2048);
            if (__all(max(max(max4(p0), max4(p1)), max4(p2)) != SENT)) break;
        }
        if (lane >= 32) p2 = u32x4{0, 0, 0, 0};
        f32x4 O[2];
#pragma unroll
        for (int ct = 0; ct < 2; ct++) {
            O[ct] = smf(p0, cat(vb[ct], U[0][ct]), f32x4{0, 0, 0, 0}, idx);
            O[ct] = smf(p1, cat(U[1][ct], U[2][ct]), O[ct], idx);
            O[ct] = smf(p2, cat(U[3][ct], u32x4{0, 0, 0, 0}), O[ct], idx);
        }
        f32x4 os;
#pragma unroll
        for (int r = 0; r < 4; r++) {
            auto sw = __builtin_amdgcn_permlane32_swap(__float_as_uint(O[0][r]), __float_as_uint(O[1][r]), false, false);
            os[r] = __uint_as_float(sw[0]) + __uint_as_float(sw[1]);
        }
        *(LAS f32x4*)(red + ((w * 32 + 16 * (lane >> 5) + r16) * 8 + 4 * (g & 1)) * 4) = os;
        bar();
        {
            const char* rp = red + ((8 * w + mc) * 8 + mi) * 4;
            float o = (*(LAS float*)(rp) + *(LAS float*)(rp + 1024)) + (*(LAS float*)(rp + 2048) + *(LAS float*)(rp + 3072));
            unsigned ob = cvtpk(o, dppf<DPP_XOR1>(o));
            if ((mc & 1) == 0) {
                bs32<AUX_SC1>(ob, ws, moff, m_slot((s + 1) % 3, sel));
                bs32<AUX_SC1>(ob, ws, moff, MH_OFF + (s + 1) * 8192);
            }
        }
    }
}

__global__ __launch_bounds__(256, 1) void k_bulk(Params p) {
    __shared__ __attribute__((aligned(16))) char smem[4 * 28672 + 32 * 912];
    const int lane = threadIdx.x & 63, w = __builtin_amdgcn_readfirstlane(threadIdx.x >> 6);
    rsrc_t ws = mkrsrc(p.ws, WS_END);
    const int b = blockIdx.x;
    if (b < 64) ffn_item<0>(p, ws, b, w, lane, smem);
    else { const int i = b - 64; u_item<0>(p, ws, i >> 5, (i >> 1) & 15, i & 1, w, lane); }
}
__global__ __launch_bounds__(256, 1) void k_chain(Params p) {
    __shared__ __attribute__((aligned(16))) char smem[16384 + 65536];
    const int lane = threadIdx.x & 63, w = __builtin_amdgcn_readfirstlane(threadIdx.x >> 6);
    rsrc_t ws = mkrsrc(p.ws, WS_END);
    if (blockIdx.x & 7) return;
    const int c = blockIdx.x >> 3;
    if (c < 16) score_cu(p, ws, c, w, lane, smem, 0);
    else if (c < 30) value_cu(p, ws, c - 16, w, lane, smem, 0);
}
__global__ __launch_bounds__(256, 1) void k_xa(Params p) {
    const int lane = threadIdx.x & 63, w = __builtin_amdgcn_readfirstlane(threadIdx.x >> 6);
    rsrc_t ws = mkrsrc(p.ws, WS_END);
    xa_item<0>(p, ws, blockIdx.x >> 4, blockIdx.x & 15, w, lane);
}
__global__ __launch_bounds__(256, 1) void k_xb(Params p) {
    __shared__ __attribute__((aligned(16))) char smem[57344];
    const int lane = threadIdx.x & 63, w = __builtin_amdgcn_readfirstlane(threadIdx.x >> 6);
    rsrc_t ws = mkrsrc(p.ws, WS_END);
    xb_item<0>(p, ws, blockIdx.x >> 2, blockIdx.x & 3, w, lane, smem);
}

extern "C" void kernel_launch(void* const* d_in, const int* in_sizes, int n_in, void* d_out, int out_size, void* d_ws, size_t ws_size,
                              hipStream_t stream) {
    Params p;
    p.x = (const u16*)d_in[0]; p.mem0 = (const u16*)d_in[1]; p.Wq = (const u16*)d_in[2]; p.Wk = (const u16*)d_in[3];
    p.Wv = (const u16*)d_in[4]; p.Wo = (const u16*)d_in[5]; p.w1 = (const u16*)d_in[8]; p.w2 = (const u16*)d_in[9];
    p.out = (u16*)d_out;
    p.ws = (char*)(((uintptr_t)d_ws + 524287) & ~(uintptr_t)524287);
    k_prep<<<256, 256, 0, stream>>>(p);
    k_bulk<<<64 + 256, 256, 0, stream>>>(p);
    k_chain<<<256, 256, 0, stream>>>(p);
    k_xa<<<256, 256, 0, stream>>>(p);
    k_xb<<<256, 256, 0, stream>>>(p);
}
```

```cpp
#include <hip/hip_runtime.h>
#include <stdint.h>

typedef unsigned short u16;
typedef __bf16 bf16x2 __attribute__((ext_vector_type(2)));
typedef __bf16 bf16x8 __attribute__((ext_vector_type(8)));
typedef __bf16 bf16x16 __attribute__((ext_vector_type(16)));
typedef short s16x4 __attribute__((ext_vector_type(4)));
typedef float f32x2 __attribute__((ext_vector_type(2)));
typedef float f32x4 __attribute__((ext_vector_type(4)));
typedef unsigned u32x2 __attribute__((ext_vector_type(2)));
typedef unsigned u32x4 __attribute__((ext_vector_type(4)));
typedef unsigned u32x8 __attribute__((ext_vector_type(8)));
#define DI __device__ __forceinline__
#define LAS __attribute__((address_space(3)))
#define GAS __attribute__((address_space(1)))
typedef __amdgpu_buffer_rsrc_t rsrc_t;

#define S_ 2048
#define D_ 448
#define NSEG 64

constexpr int RING_OFF = 0;
constexpr int XN_OFF = 512 * 1024;
constexpr int XI_OFF = XN_OFF + S_ * D_ * 2;
constexpr int AIMG_OFF = XI_OFF + S_ * D_ * 2;
constexpr int BIMG_OFF = AIMG_OFF + 16 * 28 * 14 * 1024;
constexpr int UIMG_OFF = BIMG_OFF + 16 * 28 * 14 * 1024;
constexpr int OB_OFF = UIMG_OFF + NSEG * 16 * 28 * 1024;
constexpr int MH_OFF = OB_OFF + S_ * D_ * 2;
constexpr int VM_OFF = MH_OFF + (NSEG + 1) * 8192 + 114688;
constexpr int PX_OFF = VM_OFF + NSEG * 4 * 28 * 1024;
constexpr int PM_OFF = PX_OFF + NSEG * 16 * 2048;
constexpr int XK_OFF = PM_OFF + NSEG * 8 * 1024;
constexpr int FLAG_OFF = XK_OFF + NSEG * 28672;
constexpr int UPROG_OFF = FLAG_OFF + 1024;
constexpr int FFLAG_OFF = FLAG_OFF + 2048;
constexpr int CAL_OFF = FLAG_OFF + 16384;
constexpr int WS_END = FLAG_OFF + 65536;
constexpr int DBG_OFF = WS_END;
constexpr int TS2_OFF = (WS_END + 655360 + 32767) / 32768 * 32768;
constexpr int WS_SIZE = TS2_OFF + 524288;
constexpr unsigned SENT = 0xFFFFFFFFu;
constexpr int AUX_SC1 = 16;

struct Params {
    char* ws; const u16* x; const u16* mem0; const u16* Wq; const u16* Wk; const u16* Wv; const u16* Wo;
    const u16* w1; const u16* w2; u16* out;
};

DI float b2f(unsigned v) { return __uint_as_float(v << 16); }
DI float blo(unsigned v) { return __uint_as_float(v << 16); }
DI float bhi(unsigned v) { return __uint_as_float(v & 0xFFFF0000u); }
DI unsigned cvtpk(float lo, float hi) {
    f32x2 v = {lo, hi};
    return __builtin_bit_cast(unsigned, __builtin_convertvector(v, bf16x2));
}
DI float rb(float f) { return blo(cvtpk(f, f)); }
DI f32x4 mfma32(u32x4 a, u32x4 b, f32x4 c) {
    return __builtin_amdgcn_mfma_f32_16x16x32_bf16(__builtin_bit_cast(bf16x8, a), __builtin_bit_cast(bf16x8, b), c, 0, 0, 0);
}
DI f32x4 mfma16(u32x2 a, u32x2 b, f32x4 c) {
    return __builtin_amdgcn_mfma_f32_16x16x16bf16_1k(__builtin_bit_cast(s16x4, a), __builtin_bit_cast(s16x4, b), c, 0, 0, 0);
}
DI f32x4 smf(u32x4 a, u32x8 b, f32x4 c, int idx) {
    return __builtin_amdgcn_smfmac_f32_16x16x64_bf16(__builtin_bit_cast(bf16x8, a), __builtin_bit_cast(bf16x16, b), c, idx, 0, 0);
}
DI u32x8 cat(u32x4 a, u32x4 b) { return __builtin_shufflevector(a, b, 0, 1, 2, 3, 4, 5, 6, 7); }
DI rsrc_t mkrsrc(const void* p, int bytes) { return __builtin_amdgcn_make_buffer_rsrc((void*)p, 0, bytes, 0x00020000); }
template <int AUX> DI u32x4 bl128(rsrc_t r, int vo, int so) { return __builtin_amdgcn_raw_buffer_load_b128(r, vo, so, AUX); }
template <int AUX> DI unsigned bl32(rsrc_t r, int vo, int so) { return __builtin_amdgcn_raw_buffer_load_b32(r, vo, so, AUX); }
template <int AUX> DI u32x2 bl64(rsrc_t r, int vo, int so) { return __builtin_amdgcn_raw_buffer_load_b64(r, vo, so, AUX); }
template <int AUX> DI void bs128(u32x4 v, rsrc_t r, int vo, int so) { __builtin_amdgcn_raw_buffer_store_b128(v, r, vo, so, AUX); }
template <int AUX> DI void bs64(u32x2 v, rsrc_t r, int vo, int so) { __builtin_amdgcn_raw_buffer_store_b64(v, r, vo, so, AUX); }
template <int AUX> DI void bs32(unsigned v, rsrc_t r, int vo, int so) { __builtin_amdgcn_raw_buffer_store_b32(v, r, vo, so, AUX); }
DI u32x4 ldg128(const void* p) { return *(const GAS u32x4*)p; }
DI u32x2 ldg64(const void* p) { return *(const GAS u32x2*)p; }
DI u32x4 lds128(const char* p) { return *(const LAS u32x4*)p; }
DI u32x2 lds64(const char* p) { return *(const LAS u32x2*)p; }
DI u32x2 ldstr(const char* p) {
    typedef short v4s __attribute__((ext_vector_type(4)));
    return __builtin_bit_cast(u32x2, __builtin_amdgcn_ds_read_tr16_b64_v4i16((LAS v4s*)p));
}
DI u32x4 cat2(u32x2 a, u32x2 b) { return __builtin_shufflevector(a, b, 0, 1, 2, 3); }
DI int lane_id() { int l; asm volatile("v_mbcnt_lo_u32_b32 %0, -1, 0\n\tv_mbcnt_hi_u32_b32 %0, -1, %0" : "=v"(l)); return l; }
DI void bar() { asm volatile("s_waitcnt lgkmcnt(0)\n\ts_barrier" ::: "memory"); }
DI void bar_vm() { asm volatile("s_waitcnt vmcnt(0) lgkmcnt(0)\n\ts_barrier" ::: "memory"); }
DI void dma16(const void* g, const char* ldsbase) {
    unsigned m = (unsigned)(uintptr_t)(const LAS char*)ldsbase;
    asm volatile("s_mov_b32 m0, %0\n\ts_nop 0\n\tglobal_load_lds_dwordx4 %1, off" ::"s"(m), "v"(g) : "memory");
}
DI void dmab(u32x4 rs, int vo, int so, unsigned ldsaddr) {
    asm volatile("s_mov_b32 m0, %0\n\ts_nop 0\n\tbuffer_load_dwordx4 %1, %2, %3 offen lds" ::"s"(ldsaddr), "v"(vo), "s"(rs), "s"(so) : "memory");
}
template <int GO, int LO> DI void dma7(u32x4 rs, int vo, int so, unsigned lds) {
    int tmp;
    asm volatile("s_add_u32 %0, %3, %5\n\ts_add_u32 m0, %4, %6\n\ts_nop 0\n\t"
                 "buffer_load_dwordx4 %1, %2, %0 offen lds\n\tbuffer_load_dwordx4 %1, %2, %0 offen offset:1024 lds\n\t"
                 "buffer_load_dwordx4 %1, %2, %0 offen offset:2048 lds\n\tbuffer_load_dwordx4 %1, %2, %0 offen offset:3072 lds\n\t"
                 "s_add_u32 %0, %0, 0x1000\n\ts_add_u32 m0, m0, 0x1000\n\ts_nop 0\n\t"
                 "buffer_load_dwordx4 %1, %2, %0 offen lds\n\tbuffer_load_dwordx4 %1, %2, %0 offen offset:1024 lds\n\t"
                 "buffer_load_dwordx4 %1, %2, %0 offen offset:2048 lds"
                 : "=&s"(tmp) : "v"(vo), "s"(rs), "s"(so), "s"(lds), "n"(GO), "n"(LO) : "memory", "scc");
}
DI void dma7v(u32x4 rs, const int* vo, int so, unsigned lds) {
    asm volatile("s_mov_b32 m0, %9\n\ts_nop 0\n\t"
                 "buffer_load_dwordx4 %0, %7, %8 offen lds\n\ts_add_u32 m0, m0, 0x400\n\ts_nop 0\n\t"
                 "buffer_load_dwordx4 %1, %7, %8 offen lds\n\ts_add_u32 m0, m0, 0x400\n\ts_nop 0\n\t"
                 "buffer_load_dwordx4 %2, %7, %8 offen lds\n\ts_add_u32 m0, m0, 0x400\n\ts_nop 0\n\t"
                 "buffer_load_dwordx4 %3, %7, %8 offen lds\n\ts_add_u32 m0, m0, 0x400\n\ts_nop 0\n\t"
                 "buffer_load_dwordx4 %4, %7, %8 offen lds\n\ts_add_u32 m0, m0, 0x400\n\ts_nop 0\n\t"
                 "buffer_load_dwordx4 %5, %7, %8 offen lds\n\ts_add_u32 m0, m0, 0x400\n\ts_nop 0\n\t"
                 "buffer_load_dwordx4 %6, %7, %8 offen lds"
                 ::"v"(vo[0]), "v"(vo[1]), "v"(vo[2]), "v"(vo[3]), "v"(vo[4]), "v"(vo[5]), "v"(vo[6]), "s"(rs), "s"(so), "s"(lds) : "memory", "scc");
}
DI void dma8v(u32x4 rs, const int* vo, int so, unsigned lds) {
    asm volatile("s_mov_b32 m0, %10\n\ts_nop 0\n\t"
                 "buffer_load_dwordx4 %0, %8, %9 offen lds\n\ts_add_u32 m0, m0, 0x400\n\ts_nop 0\n\t"
                 "buffer_load_dwordx4 %1, %8, %9 offen lds\n\ts_add_u32 m0, m0, 0x400\n\ts_nop 0\n\t"
                 "buffer_load_dwordx4 %2, %8, %9 offen lds\n\ts_add_u32 m0, m0, 0x400\n\ts_nop 0\n\t"
                 "buffer_load_dwordx4 %3, %8, %9 offen lds\n\ts_add_u32 m0, m0, 0x400\n\ts_nop 0\n\t"
                 "buffer_load_dwordx4 %4, %8, %9 offen lds\n\ts_add_u32 m0, m0, 0x400\n\ts_nop 0\n\t"
                 "buffer_load_dwordx4 %5, %8, %9 offen lds\n\ts_add_u32 m0, m0, 0x400\n\ts_nop 0\n\t"
                 "buffer_load_dwordx4 %6, %8, %9 offen lds\n\ts_add_u32 m0, m0, 0x400\n\ts_nop 0\n\t"
                 "buffer_load_dwordx4 %7, %8, %9 offen lds"
                 ::"v"(vo[0]), "v"(vo[1]), "v"(vo[2]), "v"(vo[3]), "v"(vo[4]), "v"(vo[5]), "v"(vo[6]), "v"(vo[7]), "s"(rs), "s"(so), "s"(lds) : "memory", "scc");
}
DI u32x4 mkdesc(const void* p, int bytes) { return u32x4{(unsigned)(uintptr_t)p, (unsigned)((uintptr_t)p >> 32) & 0xFFFFu, (unsigned)bytes, 0x00020000u}; }
template <int CTRL> DI float dppf(float v) { return __builtin_bit_cast(float, __builtin_amdgcn_update_dpp(0, __builtin_bit_cast(int, v), CTRL, 0xf, 0xf, true)); }
template <int CTRL> DI unsigned dppu(unsigned v) { return (unsigned)__builtin_amdgcn_update_dpp(0, (int)v, CTRL, 0xf, 0xf, true); }
DI void vmwait0() { __builtin_amdgcn_s_waitcnt(0x0F70); asm volatile("" ::: "memory"); }
#define VM0() asm volatile("s_waitcnt vmcnt(0)" ::: "memory")
DI void swap16(float v, float& a, float& b) { a = v; b = v; asm("s_nop 1\n\tv_permlane16_swap_b32 %0, %1" : "+v"(a), "+v"(b)); }
DI void swap32(float v, float& a, float& b) { a = v; b = v; asm("s_nop 1\n\tv_permlane32_swap_b32 %0, %1" : "+v"(a), "+v"(b)); }
DI float red_max(float v) { float a, b; swap16(v, a, b); swap32(fmaxf(a, b), a, b); return fmaxf(a, b); }
DI float red_sum(float v) { float a, b; swap16(v, a, b); swap32(a + b, a, b); return a + b; }
#define DPP_XOR1 0xB1
#define DPP_XOR2 0x4E
#define DPP_HMIRROR 0x141
#define DPP_ROR8 0x128

DI float wave_sum(float v) {
    v += dppf<DPP_XOR1>(v); v += dppf<DPP_XOR2>(v); v += dppf<DPP_HMIRROR>(v); v += dppf<DPP_ROR8>(v);
    const int iv = __builtin_bit_cast(int, v);
    const float a = __builtin_bit_cast(float, __builtin_amdgcn_readlane(iv, 0)), b = __builtin_bit_cast(float, __builtin_amdgcn_readlane(iv, 16));
    const float c = __builtin_bit_cast(float, __builtin_amdgcn_readlane(iv, 32)), d = __builtin_bit_cast(float, __builtin_amdgcn_readlane(iv, 48));
    return (a + b) + (c + d);
}
template <int AUX> DI void ln_row(rsrc_t ws, int row, int lane, u32x4 v) {
    float f[8];
#pragma unroll
    for (int i = 0; i < 4; i++) { f[2 * i] = blo(v[i]); f[2 * i + 1] = bhi(v[i]); }
    const bool act = lane < 56;
    float s = 0.f;
#pragma unroll
    for (int i = 0; i < 8; i++) s += f[i];
    if (!act) s = 0.f;
    const float mean = wave_sum(s) * (1.0f / D_);
    float q = 0.f;
#pragma unroll
    for (int i = 0; i < 8; i++) { float d = f[i] - mean; q += d * d; }
    if (!act) q = 0.f;
    const float var = rb(wave_sum(q) * (1.0f / D_));
    const float mb = rb(mean);
    const float den = rb(1 ? __builtin_amdgcn_sqrtf(rb(var + 1e-5f)) : sqrtf(rb(var + 1e-5f)));
    const float inv = __builtin_amdgcn_rcpf(den);
    u32x4 o;
#pragma unroll
    for (int i = 0; i < 4; i++) o[i] = 1 ? cvtpk(rb(f[2 * i] - mb) * inv, rb(f[2 * i + 1] - mb) * inv) : cvtpk(rb(f[2 * i] - mb) / den, rb(f[2 * i + 1] - mb) / den);
    const bool b = lane & 1;
    unsigned s0 = b ? o[0] : o[2], s1 = b ? o[1] : o[3];
    unsigned r0 = dppu<DPP_XOR1>(s0), r1 = dppu<DPP_XOR1>(s1);
    u32x4 q4 = b ? u32x4{r0, o[2], r1, o[3]} : u32x4{o[0], r0, o[1], r1};
    const int OOB = 0x7FFFFF00;
    bs128<AUX>(o, ws, act ? lane * 16 : OOB, XN_OFF + row * 896);
    {
        const int tk = row & 31, ab = (tk >> 2) & 1, rr = ((tk >> 3) << 2) | (tk & 3);
        bs128<AUX>(q4, ws, act ? (lane >> 2) * 1024 + ((lane & 3) * 16 + rr) * 16 : OOB, XI_OFF + (row >> 5) * 28672 + ab * 14 * 1024);
    }
    const int key = row & 31, k4 = key & 15, xt = key >> 4;
    const int r16 = (((k4 >> 1) & 1) << 3) | (((k4 >> 3) & 1) << 2) | (((k4 >> 2) & 1) << 1) | (k4 & 1);
    const int wq = lane / 14, j = lane % 14;
    const int xk = XK_OFF + (row >> 5) * 4 * 7168;
    bs128<AUX>(o, ws, act && j < 12 ? wq * 7168 + ((j >> 2) * 2 + xt) * 1024 + ((j & 3) * 16 + r16) * 16 : OOB, xk);
    const int to = act && j >= 12 ? wq * 7168 + 6144 + ((2 * (j - 12)) * 16 + r16) * 16 + xt * 8 : OOB;
    bs64<AUX>(u32x2{o[0], o[1]}, ws, to, xk);
    bs64<AUX>(u32x2{o[2], o[3]}, ws, to + 256, xk);
}

#define PSTAMP(idx) do {} while (0)
#define QDECL do {} while (0)
#define QSTAMP(i) do {} while (0)
__global__ __launch_bounds__(512, 1) void k_prep(Params p) {
    __shared__ __attribute__((aligned(16))) char smem[57344];
    constexpr int AUX = 0, AUXL = 0;
    const int b = blockIdx.x, lane = threadIdx.x & 63;
    const int w = __builtin_amdgcn_readfirstlane(threadIdx.x >> 6);
    const int g = lane >> 4, r16 = lane & 15;
    const int pr = (b & 7) * 4 + (b >> 6), img = pr & 1, h = pr >> 1, q = (b >> 4) & 3, half = (b >> 3) & 1;
    const int row = b * 8 + w;
    uintptr_t wq = (uintptr_t)p.Wq, wk = (uintptr_t)p.Wk, wv = (uintptr_t)p.Wv, wo = (uintptr_t)p.Wo;
    uintptr_t wsp = (uintptr_t)p.ws;
    uintptr_t xp = (uintptr_t)p.x, mp = (uintptr_t)p.mem0;
    asm("" : "+s"(wq), "+s"(wk), "+s"(wv), "+s"(wo), "+s"(wsp), "+s"(xp), "+s"(mp));
    rsrc_t ws = mkrsrc((const void*)wsp, WS_SIZE);
    QDECL; QSTAMP(0);
    const unsigned long long tstart = __builtin_amdgcn_s_memrealtime();
    const u32x4 xr = bl128<0>(mkrsrc((const void*)xp, 2048 * 896), lane < 56 ? lane * 16 : 0x7FFFFF00, row * 896);
    const u32x4 rs = mkdesc((const void*)(img ? wo : wk), D_ * 4096 * 2);
    const int so_s = img ? (h * 256 + 32 * w) * 896 + 224 * q : (112 * q + 14 * w) * 8192 + h * 512;
    const unsigned lds = (unsigned)(uintptr_t)(LAS char*)smem + w * 7168;
    int vo[7];
    if (img == 0) {
        const int hi = lane >> 5, bv = hi * 8192 + (((lane & 31) ^ hi) << 4);
#pragma unroll
        for (int i = 0; i < 7; i++) vo[i] = bv ^ (i * 16384 + (((14 * w + 2 * i) & 15) << 4));
    } else {
#pragma unroll
        for (int i = 0; i < 7; i++) vo[i] = lane * 16 + 3712 * i + (((8 * i + lane) * 4682) >> 16) * 672;
    }
    const int blk = 7 * half + (w < 7 ? w : 0);
    u32x4 a[8][2];
    const int drow = 16 * (r16 >> 3) + 4 * ((r16 >> 2) & 1) + (r16 & 1) + 8 * ((r16 >> 1) & 1);
    const rsrc_t ra = mkrsrc((const void*)(img ? wv : wq), w < 7 ? D_ * 4096 * 2 : 0);
    auto fetch = [&](int k0, int k1) {
#pragma unroll
        for (int ks = k0; ks < k1; ks++)
#pragma unroll
            for (int j = 0; j < 2; j++)
                a[ks][j] = bl128<0>(ra, drow * 8192 + g * 16 + j * 16384, 32 * blk * 8192 + h * 512 + 64 * ks);
    };
    dma7v(rs, vo, so_s, lds);
    const int t = b * 64 + lane;
    const u32x4 sent = {SENT, SENT, SENT, SENT};
    unsigned pf = 0, pf2 = 0, pf3 = 0;
    u32x4 m0 = sent;
    auto init = [&]() {
        constexpr int AS = 0;
        bs128<AS>(sent, ws, t * 16, MH_OFF + 8192);
        bs128<AS>(sent, ws, t * 16, MH_OFF + 8192 + 262144);
        if (t < (0 ? 1088 : 1024)) bs128<AS>(u32x4{0, 0, 0, 0}, ws, t * 16, FLAG_OFF);
#pragma unroll
        for (int k = 0; k < 10; k++) bs128<AS>(sent, ws, t * 16, PX_OFF + k * 262144);
        if (t < 3 * 7168) bs128<AS>(sent, ws, t * 16, VM_OFF + (NSEG - 3) * 114688);
        if (3 > 2 && t + 16384 < 3 * 7168) bs128<AS>(sent, ws, t * 16 + 262144, VM_OFF + (NSEG - 3) * 114688);
        if (t >= 2048) bs128<AS>(sent, ws, t * 16, RING_OFF);
        if (t < 2048) bs128<AS>(sent, ws, t * 16, RING_OFF + 262144);
    };
    if (w == 7) {
        const int within = t & 511;
        if (t < 2048 && within < 448) m0 = ldg128((const u16*)mp + (within & 7) * D_ + (within >> 3) * 8);
        unsigned long long pc;
        asm volatile("s_getpc_b64 s[40:41]\n\ts_add_u32 s40, s40, _Z6k_mainILi15EEv6Params@rel32@lo+4\n\t"
                     "s_addc_u32 s41, s41, _Z6k_mainILi15EEv6Params@rel32@hi+12\n\ts_mov_b64 %0, s[40:41]" : "=s"(pc) :: "s40", "s41", "scc");
        pf = bl32<0>(mkrsrc((const void*)pc, 60416), ((b >> 3) * 64 + lane) * 128, 0);
        if (!0) init();
    }
    QSTAMP(7);
    if (0) asm volatile("s_barrier" ::: "memory");
    fetch(0, 4);
    __builtin_amdgcn_sched_barrier(0);
    QSTAMP(6);
    ln_row<AUXL>(ws, row, lane, xr);
    QSTAMP(1);
    __builtin_amdgcn_s_waitcnt(0x0070 | ((2 * 4 + 5) & 15) | (((2 * 4 + 5) >> 4) << 14));
    asm volatile("s_barrier" ::: "memory");
    QSTAMP(2);
    fetch(4, 8);
    __builtin_amdgcn_sched_barrier(0);
    if (w < 7) {
        f32x4 acc[2][7];
#pragma unroll
        for (int j = 0; j < 2; j++)
#pragma unroll
            for (int k = 0; k < 7; k++) acc[j][k] = f32x4{0, 0, 0, 0};
        if (img == 0) {
#pragma unroll
            for (int ks = 0; ks < 8; ks++)
#pragma unroll
                for (int ft = 0; ft < 7; ft++) {
                    u32x4 bo = lds128(smem + ft * 8192 + ((r16 * 512 + ((g ^ r16) << 4)) ^ (ks << 6)));
                    acc[0][ft] = mfma32(a[ks][0], bo, acc[0][ft]);
                    acc[1][ft] = mfma32(a[ks][1], bo, acc[1][ft]);
                }
        } else {
            const char* tb = smem + (8 * g + (r16 >> 2)) * 224 + (r16 & 3) * 8;
#pragma unroll
            for (int ks = 0; ks < 8; ks++)
#pragma unroll
                for (int ct = 0; ct < 7; ct++) {
                    const char* tk = tb + ks * 7168;
                    u32x4 bo = cat2(ldstr(tk + ct * 32), ldstr(tk + 4 * 224 + ct * 32));
                    acc[0][ct] = mfma32(a[ks][0], bo, acc[0][ct]);
                    acc[1][ct] = mfma32(a[ks][1], bo, acc[1][ct]);
                }
        }
#pragma unroll
        for (int ct = 0; ct < 7; ct++) {
            u32x4 o = {cvtpk(acc[0][ct][0], acc[0][ct][1]), cvtpk(acc[0][ct][2], acc[0][ct][3]),
                       cvtpk(acc[1][ct][0], acc[1][ct][1]), cvtpk(acc[1][ct][2], acc[1][ct][3])};
            bs128<AUX>(o, ws, lane * 16, (img ? BIMG_OFF : AIMG_OFF) + ((h * 28 + 7 * q + ct) * 14 + blk) * 1024);
        }
    }
    else {
        if (0 == 1) init();
        if (t < 2048) bs128<AUXL>(m0, ws, t * 16, RING_OFF);
        if (t < 448) bs128<AUXL>(m0, ws, t * 16, MH_OFF);
        if (!0 && (b & 5) == 0) {
            const int k = (b >> 3) & 3;
            const int vo = lane < 32 ? (lane >> 3) * 2048 + (lane & 7) * 16 : 0x7FFFFF00;
            const unsigned long long tend = tstart + 720;
            unsigned best = 0xFFFFFFu;
            for (int r = 0; r < 64; r++) {
                unsigned long long t0, t1;
                asm volatile("s_waitcnt vmcnt(0)\n\ts_memtime %0\n\ts_waitcnt lgkmcnt(0)" : "=s"(t0)::"memory");
                bs128<AUX_SC1>(sent, ws, vo, RING_OFF + 32768 + k * 8192);
                asm volatile("s_waitcnt vmcnt(0)\n\ts_memtime %0\n\ts_waitcnt lgkmcnt(0)" : "=s"(t1)::"memory");
                best = min(best, (unsigned)(t1 - t0));
                if (r >= 1 && __builtin_amdgcn_s_memrealtime() > tend) break;
            }
            bs128<0>(u32x4{k == 0 ? best : 0xFFFFFFu, k == 1 ? best : 0xFFFFFFu, k == 2 ? best : 0xFFFFFFu, k == 3 ? best : 0xFFFFFFu}, ws,
                     lane == 0 ? 0 : 0x7FFFFF00, CAL_OFF + ((b >> 1) & 1) * 512 + (b >> 3) * 16);
        }
        if (0 == 2) init();
        asm volatile("" :: "v"(pf), "v"(pf2), "v"(pf3));
        PSTAMP(5);
    }
}

DI bool none(bool c) { return __builtin_amdgcn_ballot_w64(c) == 0; }
DI void nap() { __builtin_amdgcn_s_sleep(2); asm volatile("" ::: "memory"); }

#define BSTAMP(idx) do {} while (0)
#define WSTAMP(cond, idx) do {} while (0)

DI void wait_u(rsrc_t ws, int need, int lane, int& budget) {
    const int vo = lane < 28 ? lane * 16 : 0;
    while (true) {
        u32x4 f = bl128<AUX_SC1>(ws, vo, UPROG_OFF);
        const unsigned m = min(min(f[0], f[1]), min(f[2], f[3]));
        if (none(m < (unsigned)need) || budget <= 0) break;
        budget--; nap();
    }
}
DI void wait_mh(rsrc_t ws, int s, int lane, int& budget) {
    const int vo = lane < 56 ? lane * 128 : 0;
    while (true) {
        unsigned f = bl32<AUX_SC1>(ws, vo, MH_OFF + s * 8192);
        if (none(f == SENT) || budget <= 0) break;
        budget--; nap();
    }
}

DI void u_role(const Params& p, rsrc_t ws, int u, int w, int lane, char* smem) {
    const int i = u >> 2, ph = u & 3;
    const u32x4 rs = mkdesc(p.ws, WS_SIZE);
    const unsigned lds = (unsigned)(uintptr_t)(LAS char*)smem + w * 7168;
    auto issue = [&](int it) { dma7<0, 0>(rs, lane * 16, XI_OFF + ph * 28672 + w * 7168 + it * (4 * 28672), lds + (it & 3) * 28672); };
    issue(0); issue(1); issue(2);
    u32x4 bo[4][14];
#pragma unroll
    for (int k = 0; k < 4; k++)
#pragma unroll
        for (int blk = 0; blk < 14; blk++) bo[k][blk] = bl128<0>(ws, lane * 16, BIMG_OFF + ((16 * i + 4 * w + k) * 14 + blk) * 1024);
    int uo[4];
#pragma unroll
    for (int k = 0; k < 4; k++) uo[k] = UIMG_OFF + ph * 16 * 28672 + (16 * i + 4 * w + k) * 1024;
    vmwait0();
    BSTAMP(1);
    for (int it = 0; it < 16; it++) {
        if (it < 14) asm volatile("s_waitcnt vmcnt(11)" ::: "memory");
        else asm volatile("s_waitcnt vmcnt(4)" ::: "memory");
        bar();
        if (it >= 2 && w == 0) bs32<AUX_SC1>((unsigned)(it - 1), ws, lane == 0 ? u * 4 : 0x7FFFFFF0, UPROG_OFF);
        if (it + 3 < 16) issue(it + 3);
        const char* sl = smem + (it & 3) * 28672 + lane * 16;
        f32x4 acc[4][2];
#pragma unroll
        for (int k = 0; k < 4; k++) { acc[k][0] = f32x4{0, 0, 0, 0}; acc[k][1] = f32x4{0, 0, 0, 0}; }
#pragma unroll
        for (int blk = 0; blk < 14; blk++)
#pragma unroll
            for (int ab = 0; ab < 2; ab++) {
                u32x4 a = lds128(sl + (ab * 14 + blk) * 1024);
#pragma unroll
                for (int k = 0; k < 4; k++) acc[k][ab] = mfma32(a, bo[k][blk], acc[k][ab]);
            }
#pragma unroll
        for (int k = 0; k < 4; k++) {
            u32x4 o = {cvtpk(acc[k][0][0], acc[k][0][1]), cvtpk(acc[k][0][2], acc[k][0][3]), cvtpk(acc[k][1][0], acc[k][1][1]), cvtpk(acc[k][1][2], acc[k][1][3])};
            bs128<AUX_SC1>(o, ws, lane * 16, uo[k] + it * (4 * 16 * 28672));
        }
        if (it == 0) {
            VM0(); bar();
            if (w == 0) bs32<AUX_SC1>(1u, ws, lane == 0 ? u * 4 : 0x7FFFFFF0, UPROG_OFF);
        }
    }
    VM0(); bar();
    if (w == 0) bs32<AUX_SC1>(16u, ws, lane == 0 ? u * 4 : 0x7FFFFFF0, UPROG_OFF);
    BSTAMP(2);
}

DI void ffn_item(const Params& p, rsrc_t ws, int it, int w, int lane, char* smem) {
    const int g = lane >> 4, r16 = lane & 15;
    const int t0 = 32 * it;
    char* stg = smem + w * 28672;
    char* hid = smem + 4 * 28672;
    u32x4 bx[2][14];
#pragma unroll
    for (int tt = 0; tt < 2; tt++)
#pragma unroll
        for (int ks = 0; ks < 14; ks++) bx[tt][ks] = bl128<0>(ws, r16 * 896 + g * 16 + ks * 64, XN_OFF + (t0 + 16 * tt) * 896);
    f32x4 acc2[7][2];
#pragma unroll
    for (int i = 0; i < 7; i++) { acc2[i][0] = f32x4{0, 0, 0, 0}; acc2[i][1] = f32x4{0, 0, 0, 0}; }
    int vo1[7], vo2[7];
#pragma unroll
    for (int i = 0; i < 7; i++) { const int ci = i * 64 + lane, rw = ci / 14, cc = ci % 14; vo1[i] = rw * 3584 + cc * 16; vo2[i] = rw * 896 + cc * 16; }
    const char* tb = stg + (8 * g + (r16 >> 2)) * 224 + (r16 & 3) * 8;
    const u32x4 rs1 = mkdesc(p.w1, D_ * 1792 * 2), rs2 = mkdesc(p.w2, D_ * 1792 * 2);
    const unsigned stl = (unsigned)(uintptr_t)(LAS char*)stg;
    auto issue = [&](int n) {
        const int c = n / 28, ph = (n % 28) / 14, ks = n % 14;
        if (ph == 0) dma7v(rs1, vo1, (32 * ks * 1792 + 448 * c + 112 * w) * 2, stl + (n & 3) * 7168);
        else dma7v(rs2, vo2, ((448 * c + 32 * ks) * D_ + 112 * w) * 2, stl + (n & 3) * 7168);
    };
    vmwait0();
    issue(0); issue(1); issue(2);
    for (int c = 0; c < 4; c++) {
        f32x4 acc1[7][2];
#pragma unroll
        for (int i = 0; i < 7; i++) { acc1[i][0] = f32x4{0, 0, 0, 0}; acc1[i][1] = f32x4{0, 0, 0, 0}; }
#pragma unroll
        for (int ks = 0; ks < 14; ks++) {
            const int n = c * 28 + ks;
            asm volatile("s_waitcnt lgkmcnt(0)" ::: "memory");
            issue(n + 3);
            asm volatile("s_waitcnt vmcnt(21)" ::: "memory");
#pragma unroll
            for (int nt = 0; nt < 7; nt++) {
                u32x4 ao = cat2(ldstr(tb + (n & 3) * 7168 + nt * 32), ldstr(tb + (n & 3) * 7168 + 4 * 224 + nt * 32));
                acc1[nt][0] = mfma32(ao, bx[0][ks], acc1[nt][0]);
                acc1[nt][1] = mfma32(ao, bx[1][ks], acc1[nt][1]);
            }
        }
        bar();
#pragma unroll
        for (int nt = 0; nt < 7; nt++)
#pragma unroll
            for (int tt = 0; tt < 2; tt++) {
                f32x4 v = acc1[nt][tt];
                u32x2 o = {cvtpk(fmaxf(v[0], 0.f), fmaxf(v[1], 0.f)), cvtpk(fmaxf(v[2], 0.f), fmaxf(v[3], 0.f))};
                *(LAS u32x2*)(hid + (16 * tt + r16) * 912 + (112 * w + 16 * nt + 4 * g) * 2) = o;
            }
        bar();
#pragma unroll
        for (int ks = 0; ks < 14; ks++) {
            const int n = c * 28 + 14 + ks;
            asm volatile("s_waitcnt lgkmcnt(0)" ::: "memory");
            if (n + 3 < 112) { issue(n + 3); asm volatile("s_waitcnt vmcnt(21)" ::: "memory"); }
            else asm volatile("s_waitcnt vmcnt(0)" ::: "memory");
            u32x4 h0 = lds128(hid + r16 * 912 + ks * 64 + g * 16), h1 = lds128(hid + (16 + r16) * 912 + ks * 64 + g * 16);
#pragma unroll
            for (int nt = 0; nt < 7; nt++) {
                u32x4 ao = cat2(ldstr(tb + (n & 3) * 7168 + nt * 32), ldstr(tb + (n & 3) * 7168 + 4 * 224 + nt * 32));
                acc2[nt][0] = mfma32(ao, h0, acc2[nt][0]);
                acc2[nt][1] = mfma32(ao, h1, acc2[nt][1]);
            }
        }
    }
#pragma unroll
    for (int nt = 0; nt < 7; nt++)
#pragma unroll
        for (int tt = 0; tt < 2; tt++) {
            f32x4 v = acc2[nt][tt];
            u32x2 o = {cvtpk(v[0], v[1]), cvtpk(v[2], v[3])};
            bs64<AUX_SC1>(o, ws, (t0 + 16 * tt + r16) * 896 + (112 * w + 16 * nt + 4 * g) * 2, OB_OFF);
        }
    bar_vm();
    if (w == 0) bs32<AUX_SC1>(1u, ws, lane == 0 ? 0 : 0x7FFFFFF0, FFLAG_OFF + it * 4);
}

DI void xa_item(const Params& p, rsrc_t ws, int G, int h, int w, int lane, char* smem, int& budget, int st0) {
    const int g = lane >> 4, r16 = lane & 15;
    const int s = 4 * G + w, t0 = 32 * s;
    const u32x4 rs = mkdesc(p.ws, WS_SIZE);
    const unsigned lds = (unsigned)(uintptr_t)(LAS char*)smem + w * 7168;
    const int ab = AIMG_OFF + h * 28 * 14 * 1024 + w * 7168;
#define XA_ISSUE(pp) dma7<(pp) * 28672, ((pp) & 3) * 28672>(rs, lane * 16, ab, lds)
    bar();
    XA_ISSUE(0); XA_ISSUE(1); XA_ISSUE(2);
    u32x4 bx[2][14];
#pragma unroll
    for (int tt = 0; tt < 2; tt++)
#pragma unroll
        for (int blk = 0; blk < 14; blk++) bx[tt][blk] = bl128<0>(ws, lane * 16, XI_OFF + s * 28672 + (tt * 14 + blk) * 1024);
    vmwait0();
    u32x4 qb[14][2];
#define XA_STEP(pp) { \
        if ((pp) < 12) asm volatile("s_waitcnt vmcnt(14)" ::: "memory"); \
        else if ((pp) == 12) asm volatile("s_waitcnt vmcnt(7)" ::: "memory"); \
        else asm volatile("s_waitcnt vmcnt(0)" ::: "memory"); \
        bar(); \
        if ((pp) + 3 < 14) XA_ISSUE(((pp) + 3 < 14 ? (pp) + 3 : 0)); \
        const char* sl = smem + ((pp) & 3) * 28672 + lane * 16; \
        f32x4 acc[2][2] = {{{0, 0, 0, 0}, {0, 0, 0, 0}}, {{0, 0, 0, 0}, {0, 0, 0, 0}}}; \
        _Pragma("unroll") for (int blk = 0; blk < 14; blk++) \
            _Pragma("unroll") for (int a = 0; a < 2; a++) { \
                u32x4 ao = lds128(sl + (a * 14 + blk) * 1024); \
                acc[a][0] = mfma32(ao, bx[0][blk], acc[a][0]); \
                acc[a][1] = mfma32(ao, bx[1][blk], acc[a][1]); \
            } \
        _Pragma("unroll") for (int tt = 0; tt < 2; tt++) \
            qb[pp][tt] = u32x4{cvtpk(acc[0][tt][0], acc[0][tt][1]), cvtpk(acc[0][tt][2], acc[0][tt][3]), cvtpk(acc[1][tt][0], acc[1][tt][1]), cvtpk(acc[1][tt][2], acc[1][tt][3])}; \
        __builtin_amdgcn_sched_barrier(0); \
    }
    XA_STEP(0) XA_STEP(1) XA_STEP(2) XA_STEP(3) XA_STEP(4) XA_STEP(5) XA_STEP(6)
    XA_STEP(7) XA_STEP(8) XA_STEP(9) XA_STEP(10) XA_STEP(11) XA_STEP(12) XA_STEP(13)
    BSTAMP(st0);
    f32x4 sx[2][2] = {{{0, 0, 0, 0}, {0, 0, 0, 0}}, {{0, 0, 0, 0}, {0, 0, 0, 0}}};
    const int kx = (8 * (r16 >> 2) + (r16 & 3)) * 896 + g * 8;
#pragma unroll
    for (int pp = 0; pp < 14; pp++)
#pragma unroll
        for (int kt = 0; kt < 2; kt++) {
            u32x4 ka = cat2(bl64<0>(ws, kx + pp * 64, XN_OFF + (t0 + kt * 4) * 896), bl64<0>(ws, kx + pp * 64 + 32, XN_OFF + (t0 + kt * 4) * 896));
            sx[kt][0] = mfma32(ka, qb[pp][0], sx[kt][0]);
            sx[kt][1] = mfma32(ka, qb[pp][1], sx[kt][1]);
        }
    const float sc = 0.0625f * 1.4426950408889634f;
    float ex[2][8], mxx[2], sux[2];
#pragma unroll
    for (int tt = 0; tt < 2; tt++) {
        const int tok = 8 * (r16 >> 2) + (r16 & 3) + 4 * tt;
        float mx = -3.0e38f;
#pragma unroll
        for (int i = 0; i < 8; i++) {
            ex[tt][i] = 8 * g + i <= tok ? sx[i >> 2][tt][i & 3] : -3.0e38f;
            mx = fmaxf(mx, ex[tt][i]);
        }
        mx = red_max(mx);
        float sum = 0.f;
#pragma unroll
        for (int i = 0; i < 8; i++) { ex[tt][i] = ex[tt][i] > -1.0e38f ? __builtin_amdgcn_exp2f((ex[tt][i] - mx) * sc) : 0.f; sum += ex[tt][i]; }
        mxx[tt] = mx; sux[tt] = red_sum(sum);
        asm volatile("" : "+v"(ex[tt][0]), "+v"(ex[tt][1]), "+v"(ex[tt][2]), "+v"(ex[tt][3]), "+v"(ex[tt][4]), "+v"(ex[tt][5]), "+v"(ex[tt][6]),
                     "+v"(ex[tt][7]), "+v"(mxx[tt]), "+v"(sux[tt]));
    }
    if (0 && s >= NSEG - 0) {
        wait_mh(ws, s - 1, lane, budget);
        const u32x4 rs2 = mkdesc(p.ws, WS_SIZE);
        const int vo = MH_OFF + s * 8192 + (lane < 56 ? lane * 128 : 0);
        unsigned f = SENT;
        for (int n = 0; n < 4096; n++) {
            asm volatile("buffer_load_dword %0, %1, %2, 0 offen sc1\n\ts_waitcnt vmcnt(3)\n\ts_sleep 1" : "+v"(f) : "v"(vo), "s"(rs2) : "memory");
            if (none(f == SENT)) break;
        }
        VM0();
    }
    const int mo = (g >> 1) * 128 + (r16 & 7) * 16 + (g & 1) * 8;
    u32x4 km[14];
    while (true) {
        unsigned mxu = 0;
#pragma unroll
        for (int pp = 0; pp < 14; pp++)
            km[pp] = cat2(bl64<AUX_SC1>(ws, mo + pp * 512, MH_OFF + s * 8192), bl64<AUX_SC1>(ws, mo + pp * 512 + 256, MH_OFF + s * 8192));
#pragma unroll
        for (int pp = 0; pp < 14; pp++) mxu = max(max(mxu, max(km[pp][0], km[pp][1])), max(km[pp][2], km[pp][3]));
        if (none(mxu == SENT) || budget <= 0) break;
        budget--; nap();
    }
    WSTAMP(G >= 14 && w == 3, 16);
    f32x4 sm[2][2] = {{{0, 0, 0, 0}, {0, 0, 0, 0}}, {{0, 0, 0, 0}, {0, 0, 0, 0}}};
#pragma unroll
    for (int pp = 0; pp < 14; pp++) {
        sm[pp & 1][0] = mfma32(km[pp], qb[pp][0], sm[pp & 1][0]);
        sm[pp & 1][1] = mfma32(km[pp], qb[pp][1], sm[pp & 1][1]);
    }
#pragma unroll
    for (int tt = 0; tt < 2; tt++) {
        float vm[4];
        float mx = mxx[tt];
#pragma unroll
        for (int r = 0; r < 4; r++) { vm[r] = g < 2 ? sm[0][tt][r] + sm[1][tt][r] : -3.0e38f; mx = fmaxf(mx, vm[r]); }
        mx = red_max(mx);
        const float f = __builtin_amdgcn_exp2f((mxx[tt] - mx) * sc);
        float sum = 0.f;
#pragma unroll
        for (int r = 0; r < 4; r++) { vm[r] = g < 2 ? __builtin_amdgcn_exp2f((vm[r] - mx) * sc) : 0.f; sum += vm[r]; }
        sum = red_sum(sum) + sux[tt] * f;
        const float inv = 1.0f / sum, fx = f * inv;
        const float* vx = ex[tt];
        u32x4 o = {cvtpk(vx[0] * fx, vx[1] * fx), cvtpk(vx[2] * fx, vx[3] * fx), cvtpk(vx[4] * fx, vx[5] * fx), cvtpk(vx[6] * fx, vx[7] * fx)};
        bs128<AUX_SC1>(o, ws, lane * 16, PX_OFF + ((s * 16 + h) * 2 + tt) * 1024);
        u32x2 om = {cvtpk(vm[0] * inv, vm[1] * inv), cvtpk(vm[2] * inv, vm[3] * inv)};
        bs64<AUX_SC1>(om, ws, g < 2 ? r16 * 16 + g * 8 : 0x7FFFFF00, PM_OFF + ((s * 16 + h) * 2 + tt) * 256);
    }
    BSTAMP(st0 + 1);
}

template <int NT> DI void xb_item(const Params& p, rsrc_t ws, int s, int ct0, int w, int lane, char* smem, int& budget, int st0) {
    constexpr int CNT = 128 * NT, NN = (CNT + 255) / 256;
    f32x4 acc[NT][2];
#pragma unroll
    for (int i = 0; i < NT; i++) { acc[i][0] = f32x4{0, 0, 0, 0}; acc[i][1] = f32x4{0, 0, 0, 0}; }
    wait_u(ws, (s >> 2) + 1, lane, budget);
    u32x4 u[4][NT];
#pragma unroll
    for (int hh = 0; hh < 4; hh++)
#pragma unroll
        for (int ct = 0; ct < NT; ct++) u[hh][ct] = bl128<AUX_SC1>(ws, lane * 16, UIMG_OFF + ((s * 16 + 4 * w + hh) * 28 + ct0 + ct) * 1024);
    while (true) {
        unsigned f = bl32<AUX_SC1>(ws, 0, FFLAG_OFF + s * 4);
        if (none(f == 0u) || budget <= 0) break;
        budget--; nap();
    }
    u32x2 xv[NN], ob[NN];
    int eo[NN];
#pragma unroll
    for (int n = 0; n < NN; n++) {
        const int i = n * 256 + w * 64 + lane, l = i & 63, tt = (i >> 6) & 1, ct = i >> 7;
        const int t = 32 * s + 8 * ((l & 15) >> 2) + (l & 3) + 4 * tt, c = 16 * (ct0 + ct) + 4 * (l >> 4);
        eo[n] = i < CNT ? (t * D_ + c) * 2 : 0;
        xv[n] = ldg64((const char*)p.x + eo[n]);
        ob[n] = bl64<AUX_SC1>(ws, eo[n], OB_OFF);
    }
    WSTAMP(s >= 48, 20 + w);
    const bool vdir = s >= NSEG - 3;
    wait_mh(ws, vdir ? min(s + 1, NSEG - 1 - 1) : min(s + 2, NSEG), lane, budget);
    if (1 && NT < 7) {
        if (s == NSEG - 1) __builtin_amdgcn_s_sleep(88);
        else __builtin_amdgcn_s_sleep(40);
    }
    u32x4 v[NT], px[4][2], pm[2];
    const int pmo = (lane >> 5) * 1024 + (lane & 15) * 16 + ((lane >> 4) & 1) * 8;
    while (true) {
        unsigned mxu = 0;
#pragma unroll
        for (int ct = 0; ct < NT; ct++) v[ct] = bl128<AUX_SC1>(ws, lane * 16, VM_OFF + ((s * 4 + w) * 28 + ct0 + ct) * 1024);
#pragma unroll
        for (int hh = 0; hh < 4; hh++) {
            px[hh][0] = bl128<AUX_SC1>(ws, lane * 16, PX_OFF + ((s * 16 + 4 * w + hh) * 2) * 1024);
            px[hh][1] = bl128<AUX_SC1>(ws, lane * 16, PX_OFF + ((s * 16 + 4 * w + hh) * 2 + 1) * 1024);
        }
#pragma unroll
        for (int tt = 0; tt < 2; tt++) {
            const u32x2 e = bl64<AUX_SC1>(ws, pmo + tt * 256, PM_OFF + (s * 16 + 4 * w) * 512), o = bl64<AUX_SC1>(ws, pmo + tt * 256 + 512, PM_OFF + (s * 16 + 4 * w) * 512);
            pm[tt] = u32x4{e[0], o[0], e[1], o[1]};
        }
#pragma unroll
        for (int ct = 0; ct < NT; ct++) mxu = max(max(mxu, max(v[ct][0], v[ct][1])), max(v[ct][2], v[ct][3]));
        if (!vdir) mxu = 0;
#pragma unroll
        for (int r = 0; r < 4; r++) {
#pragma unroll
            for (int hh = 0; hh < 4; hh++) mxu = max(mxu, max(px[hh][0][r], px[hh][1][r]));
            mxu = max(mxu, max(pm[0][r], pm[1][r]));
        }
        if (none(mxu == SENT) || budget <= 0) break;
        budget--; nap();
    }
    WSTAMP(s >= 48, 24 + w);
#pragma unroll
    for (int hh = 0; hh < 4; hh++)
#pragma unroll
        for (int ct = 0; ct < NT; ct++) {
            acc[ct][0] = mfma32(u[hh][ct], px[hh][0], acc[ct][0]);
            acc[ct][1] = mfma32(u[hh][ct], px[hh][1], acc[ct][1]);
        }
#pragma unroll
    for (int ct = 0; ct < NT; ct++) {
        acc[ct][0] = mfma32(v[ct], pm[0], acc[ct][0]);
        acc[ct][1] = mfma32(v[ct], pm[1], acc[ct][1]);
    }
#pragma unroll
    for (int ct = 0; ct < NT; ct++)
#pragma unroll
        for (int tt = 0; tt < 2; tt++) *(LAS f32x4*)(smem + ((w * NT + ct) * 128 + tt * 64 + lane) * 16) = acc[ct][tt];
    bar();
#pragma unroll
    for (int n = 0; n < NN; n++) {
        const int i = n * 256 + w * 64 + lane;
        if (i < CNT) {
            f32x4 o = *(LAS f32x4*)(smem + i * 16);
#pragma unroll
            for (int ww = 1; ww < 4; ww++) o += *(LAS f32x4*)(smem + (ww * CNT + i) * 16);
            u32x2 a = {cvtpk(o[0], o[1]), cvtpk(o[2], o[3])};
            u32x2 r = {cvtpk(blo(xv[n][0]) + blo(a[0]), bhi(xv[n][0]) + bhi(a[0])), cvtpk(blo(xv[n][1]) + blo(a[1]), bhi(xv[n][1]) + bhi(a[1]))};
            u32x2 y = {cvtpk(blo(r[0]) + blo(ob[n][0]), bhi(r[0]) + bhi(ob[n][0])), cvtpk(blo(r[1]) + blo(ob[n][1]), bhi(r[1]) + bhi(ob[n][1]))};
            *(GAS u32x2*)((char*)p.out + eo[n]) = y;
        }
    }
    bar();
    BSTAMP(st0);
}

#define RFL(x) __builtin_amdgcn_readfirstlane(x)
DI void time_ring(rsrc_t ws, int slot, int w, int lane) {
    const u32x4 sent = {SENT, SENT, SENT, SENT};
    const int vo = lane < 32 ? (lane >> 3) * 2048 + (lane & 7) * 16 : 0x7FFFFF00;
    unsigned best = 0xFFFFFFu;
    for (int r = 0; r < 2; r++) {
        unsigned long long ta, tb;
        asm volatile("s_waitcnt vmcnt(0)\n\ts_memtime %0\n\ts_waitcnt lgkmcnt(0)" : "=s"(ta)::"memory");
        bs128<AUX_SC1>(sent, ws, vo, RING_OFF + 32768 + w * 8192);
        asm volatile("s_waitcnt vmcnt(0)\n\ts_memtime %0\n\ts_waitcnt lgkmcnt(0)" : "=s"(tb)::"memory");
        best = min(best, (unsigned)(tb - ta));
    }
    bs32<AUX_SC1>(best | 1, ws, lane == 0 ? 0 : 0x7FFFFF00, CAL_OFF + slot * 16 + w * 4);
}
DI void calibrate(rsrc_t ws, int w, int lane) {
    if (w != 0) return;
    const bool used = !0 || (lane & 31) < 14;
    u32x4 v = bl128<AUX_SC1>(ws, lane * 16, CAL_OFF);
    for (int budget = 1 << 17; 0 && budget > 0; budget--) {
        if (none(used && (v[0] == 0 || v[1] == 0 || v[2] == 0 || v[3] == 0))) break;
        nap();
        v = bl128<AUX_SC1>(ws, lane * 16, CAL_OFF);
    }
    unsigned t[4];
#pragma unroll
    for (int k = 0; k < 4; k++) {
        unsigned m = used ? v[k] : 0xFFFFFFu;
#pragma unroll
        for (int o = 16; o; o >>= 1) m = min(m, (unsigned)__shfl_xor((int)m, o));
        t[k] = __builtin_amdgcn_readlane(m, 0) + __builtin_amdgcn_readlane(m, 32);
    }
    unsigned b0 = t[0], sel = 0;
#pragma unroll
    for (int i = 1; i < 4; i++) if (t[i] < b0) { b0 = t[i]; sel = i; }
    bs128<AUX_SC1>(u32x4{t[3], 0, 0, 0}, ws, lane == 0 ? 16 : 0x7FFFFFF0, FLAG_OFF);
    bs128<AUX_SC1>(u32x4{sel + 1, t[0], t[1], t[2]}, ws, lane == 0 ? 0 : 0x7FFFFFF0, FLAG_OFF);
}

DI void refresher(const Params& p, rsrc_t ws, int rw, int lane) {
    unsigned sel = 0;
    for (int n = 0; n < (1 << 14) && sel == 0; n++) { sel = RFL(bl32<AUX_SC1>(ws, 0, FLAG_OFF)); if (sel == 0) nap(); }
    sel = (sel - 1) & 3;
    const int t = rw * 63 + lane;
    int vo = 0x7FFFFFF0;
    if (lane == 63) vo = MH_OFF + NSEG * 8192;
    else if (t < 168) vo = (t / 56) * 32768 + sel * 8192 + (t % 56) * 128;
    else if (t < 408) vo = (3 + (t - 168) / 40) * 32768 + sel * 8192 + ((t - 168) % 40) * 128;
    const u32x4 rs = mkdesc(p.ws, WS_SIZE);
    unsigned v = SENT;
    for (int n = 0; n < (1 << 15); n++) {
        asm volatile("buffer_load_dword %0, %1, %2, 0 offen sc1\n\ts_waitcnt vmcnt(6)\n\ts_sleep 1" : "+v"(v) : "v"(vo), "s"(rs) : "memory");
        if (__builtin_amdgcn_readlane(v, 63) != SENT) break;
    }
    VM0();
}

DI void u_prefetch(rsrc_t ws, int pw, int lane) {
    int budget = 1 << 17;
    for (int s = 1; s < NSEG; s++) {
        if (s < 4 * 16 && (s & 3) == 0 || s == 1) wait_u(ws, (s >> 2) + 1, lane, budget);
        if (s > 0) {
            while (true) {
                unsigned f = RFL(bl32<AUX_SC1>(ws, 0, MH_OFF + (s - 0) * 8192 + (pw % 56) * 128));
                if (f != SENT || budget <= 0) break;
                budget--; __builtin_amdgcn_s_sleep(4);
            }
        }
        for (int k = 0; k < (pw & 7); k++) __builtin_amdgcn_s_sleep(4);
        const unsigned d = bl32<16>(ws, lane < 56 ? lane * 128 : 0x7FFFFFF0, UIMG_OFF + (s * 448 + pw * 7) * 1024);
        asm volatile("" :: "v"(d));
    }
}

DI int kb_pack(int w) {
    int kbq[7];
    kbq[0] = w; kbq[1] = w < 3 ? w + 4 : 6;
    int c = 0, pk = kbq[0] | (kbq[1] << 4);
#pragma unroll
    for (int q = 2; q < 7; q++) {
        if (c == kbq[0]) c++;
        if (c == kbq[1]) c++;
        pk |= c << (4 * q); c++;
    }
    return pk;
}
DI int taddr(int f, int slot) { return (f >> 3) * 256 + ((((f >> 2) & 1) ^ ((f >> 3) & 1)) << 7) + (f & 3) * 32 + ((slot ^ ((f >> 2) & 3)) << 3); }

DI void score_cu(const Params& p, int h, int w, int lane, char* smem) {
    const unsigned lds = (unsigned)(uintptr_t)(LAS char*)smem;
    const unsigned Tb = lds + 8192 + w * 3584, Sb = lds + 22528;
    const int g = lane >> 4, r16 = lane & 15, i = lane >> 3, c = lane & 7;
    const int pk = kb_pack(w);
    const int hh = h & 3, grp = h >> 2;
    const unsigned wslo = (unsigned)(uintptr_t)p.ws, wshi = (unsigned)((uintptr_t)p.ws >> 32);
    const unsigned tr1 = Tb + taddr(8 * g + (r16 >> 2), r16 & 3);
    asm volatile(
    "s_mov_b32 s40, %[wslo]\n"
    "s_and_b32 s41, %[wshi], 0xffff\n"
    "s_mov_b32 s42, %[wsend]\n"
    "s_mov_b32 s43, 0x20000\n"
    "s_mov_b32 s44, %[wbase]\n"
    "s_bfe_u32 s45, %[kbpack], 0x40000\n"
    "s_lshl_b32 s45, s45, 10\n"
    "s_add_u32 s52, s45, %[ldsbase]\n"
    "s_bfe_u32 s46, %[kbpack], 0x40004\n"
    "s_lshl_b32 s46, s46, 10\n"
    "s_add_u32 s53, s46, %[ldsbase]\n"
    "s_bfe_u32 s47, %[kbpack], 0x40008\n"
    "s_lshl_b32 s47, s47, 10\n"
    "s_add_u32 s54, s47, %[ldsbase]\n"
    "s_bfe_u32 s48, %[kbpack], 0x4000c\n"
    "s_lshl_b32 s48, s48, 10\n"
    "s_add_u32 s55, s48, %[ldsbase]\n"
    "s_bfe_u32 s49, %[kbpack], 0x40010\n"
    "s_lshl_b32 s49, s49, 10\n"
    "s_add_u32 s56, s49, %[ldsbase]\n"
    "s_bfe_u32 s50, %[kbpack], 0x40014\n"
    "s_lshl_b32 s50, s50, 10\n"
    "s_add_u32 s57, s50, %[ldsbase]\n"
    "s_bfe_u32 s51, %[kbpack], 0x40018\n"
    "s_lshl_b32 s51, s51, 10\n"
    "s_add_u32 s58, s51, %[ldsbase]\n"
    "s_mov_b32 s70, 0\n"
    "s_mov_b32 s76, 0x4000\n"
    "s_mov_b32 s77, %[scale]\n"
    "s_mov_b32 s82, %[wv]\n"
    "s_mov_b32 s67, %[xk]\n"
    "s_mov_b32 s68, %[upbase]\n"
    "s_mov_b32 s72, 0\n"
    "s_mov_b32 s83, 0\n"
    "s_mov_b32 s96, %[dbg]\n"
    "buffer_load_dwordx4 v[80:83], %[l16], s[40:43], s67 offen\n"
    "buffer_load_dwordx4 v[84:87], %[l16], s[40:43], s67 offen offset:1024\n"
    "buffer_load_dwordx4 v[88:91], %[l16], s[40:43], s67 offen offset:2048\n"
    "buffer_load_dwordx4 v[92:95], %[l16], s[40:43], s67 offen offset:3072\n"
    "s_add_u32 s79, s67, 0x1000\n"
    "buffer_load_dwordx4 v[96:99], %[l16], s[40:43], s79 offen\n"
    "buffer_load_dwordx4 v[100:103], %[l16], s[40:43], s79 offen offset:1024\n"
    "buffer_load_dwordx4 v[104:107], %[l16], s[40:43], s79 offen offset:2048\n"
    "s_add_u32 s67, s67, 0x7000\n"
    "s_lshl_b32 s78, s45, 1\n"
    "s_add_u32 s78, s78, s44\n"
    "buffer_load_dwordx4 a[0:3], %[l16], s[40:43], s78 offen\n"
    "buffer_load_dwordx4 a[4:7], %[l16], s[40:43], s78 offen offset:1024\n"
    "s_add_u32 s78, s78, 0x3800\n"
    "buffer_load_dwordx4 a[8:11], %[l16], s[40:43], s78 offen\n"
    "buffer_load_dwordx4 a[12:15], %[l16], s[40:43], s78 offen offset:1024\n"
    "s_add_u32 s78, s78, 0x3800\n"
    "buffer_load_dwordx4 a[16:19], %[l16], s[40:43], s78 offen\n"
    "buffer_load_dwordx4 a[20:23], %[l16], s[40:43], s78 offen offset:1024\n"
    "s_add_u32 s78, s78, 0x3800\n"
    "buffer_load_dwordx4 a[24:27], %[l16], s[40:43], s78 offen\n"
    "buffer_load_dwordx4 a[28:31], %[l16], s[40:43], s78 offen offset:1024\n"
    "s_add_u32 s78, s78, 0x3800\n"
    "buffer_load_dwordx4 a[32:35], %[l16], s[40:43], s78 offen\n"
    "buffer_load_dwordx4 a[36:39], %[l16], s[40:43], s78 offen offset:1024\n"
    "s_add_u32 s78, s78, 0x3800\n"
    "buffer_load_dwordx4 a[40:43], %[l16], s[40:43], s78 offen\n"
    "buffer_load_dwordx4 a[44:47], %[l16], s[40:43], s78 offen offset:1024\n"
    "s_add_u32 s78, s78, 0x3800\n"
    "buffer_load_dwordx4 a[48:51], %[l16], s[40:43], s78 offen\n"
    "buffer_load_dwordx4 a[52:55], %[l16], s[40:43], s78 offen offset:1024\n"
    "s_lshl_b32 s78, s46, 1\n"
    "s_add_u32 s78, s78, s44\n"
    "buffer_load_dwordx4 a[56:59], %[l16], s[40:43], s78 offen\n"
    "buffer_load_dwordx4 a[60:63], %[l16], s[40:43], s78 offen offset:1024\n"
    "s_add_u32 s78, s78, 0x3800\n"
    "buffer_load_dwordx4 a[64:67], %[l16], s[40:43], s78 offen\n"
    "buffer_load_dwordx4 a[68:71], %[l16], s[40:43], s78 offen offset:1024\n"
    "s_add_u32 s78, s78, 0x3800\n"
    "buffer_load_dwordx4 a[72:75], %[l16], s[40:43], s78 offen\n"
    "buffer_load_dwordx4 a[76:79], %[l16], s[40:43], s78 offen offset:1024\n"
    "s_add_u32 s78, s78, 0x3800\n"
    "buffer_load_dwordx4 a[80:83], %[l16], s[40:43], s78 offen\n"
    "buffer_load_dwordx4 a[84:87], %[l16], s[40:43], s78 offen offset:1024\n"
    "s_add_u32 s78, s78, 0x3800\n"
    "buffer_load_dwordx4 a[88:91], %[l16], s[40:43], s78 offen\n"
    "buffer_load_dwordx4 a[92:95], %[l16], s[40:43], s78 offen offset:1024\n"
    "s_add_u32 s78, s78, 0x3800\n"
    "buffer_load_dwordx4 a[96:99], %[l16], s[40:43], s78 offen\n"
    "buffer_load_dwordx4 a[100:103], %[l16], s[40:43], s78 offen offset:1024\n"
    "s_add_u32 s78, s78, 0x3800\n"
    "buffer_load_dwordx4 a[104:107], %[l16], s[40:43], s78 offen\n"
    "buffer_load_dwordx4 a[108:111], %[l16], s[40:43], s78 offen offset:1024\n"
    "s_lshl_b32 s78, s47, 1\n"
    "s_add_u32 s78, s78, s44\n"
    "buffer_load_dwordx4 a[112:115], %[l16], s[40:43], s78 offen\n"
    "buffer_load_dwordx4 a[116:119], %[l16], s[40:43], s78 offen offset:1024\n"
    "s_add_u32 s78, s78, 0x3800\n"
    "buffer_load_dwordx4 a[120:123], %[l16], s[40:43], s78 offen\n"
    "buffer_load_dwordx4 a[124:127], %[l16], s[40:43], s78 offen offset:1024\n"
    "s_add_u32 s78, s78, 0x3800\n"
    "buffer_load_dwordx4 a[128:131], %[l16], s[40:43], s78 offen\n"
    "buffer_load_dwordx4 a[132:135], %[l16], s[40:43], s78 offen offset:1024\n"
    "s_add_u32 s78, s78, 0x3800\n"
    "buffer_load_dwordx4 a[136:139], %[l16], s[40:43], s78 offen\n"
    "buffer_load_dwordx4 a[140:143], %[l16], s[40:43], s78 offen offset:1024\n"
    "s_add_u32 s78, s78, 0x3800\n"
    "buffer_load_dwordx4 a[144:147], %[l16], s[40:43], s78 offen\n"
    "buffer_load_dwordx4 a[148:151], %[l16], s[40:43], s78 offen offset:1024\n"
    "s_add_u32 s78, s78, 0x3800\n"
    "buffer_load_dwordx4 a[152:155], %[l16], s[40:43], s78 offen\n"
    "buffer_load_dwordx4 a[156:159], %[l16], s[40:43], s78 offen offset:1024\n"
    "s_add_u32 s78, s78, 0x3800\n"
    "buffer_load_dwordx4 a[160:163], %[l16], s[40:43], s78 offen\n"
    "buffer_load_dwordx4 a[164:167], %[l16], s[40:43], s78 offen offset:1024\n"
    "s_lshl_b32 s78, s48, 1\n"
    "s_add_u32 s78, s78, s44\n"
    "buffer_load_dwordx4 a[168:171], %[l16], s[40:43], s78 offen\n"
    "buffer_load_dwordx4 a[172:175], %[l16], s[40:43], s78 offen offset:1024\n"
    "s_add_u32 s78, s78, 0x3800\n"
    "buffer_load_dwordx4 a[176:179], %[l16], s[40:43], s78 offen\n"
    "buffer_load_dwordx4 a[180:183], %[l16], s[40:43], s78 offen offset:1024\n"
    "s_add_u32 s78, s78, 0x3800\n"
    "buffer_load_dwordx4 a[184:187], %[l16], s[40:43], s78 offen\n"
    "buffer_load_dwordx4 a[188:191], %[l16], s[40:43], s78 offen offset:1024\n"
    "s_add_u32 s78, s78, 0x3800\n"
    "buffer_load_dwordx4 a[192:195], %[l16], s[40:43], s78 offen\n"
    "buffer_load_dwordx4 a[196:199], %[l16], s[40:43], s78 offen offset:1024\n"
    "s_add_u32 s78, s78, 0x3800\n"
    "buffer_load_dwordx4 a[200:203], %[l16], s[40:43], s78 offen\n"
    "buffer_load_dwordx4 a[204:207], %[l16], s[40:43], s78 offen offset:1024\n"
    "s_add_u32 s78, s78, 0x3800\n"
    "buffer_load_dwordx4 a[208:211], %[l16], s[40:43], s78 offen\n"
    "buffer_load_dwordx4 a[212:215], %[l16], s[40:43], s78 offen offset:1024\n"
    "s_add_u32 s78, s78, 0x3800\n"
    "buffer_load_dwordx4 a[216:219], %[l16], s[40:43], s78 offen\n"
    "buffer_load_dwordx4 a[220:223], %[l16], s[40:43], s78 offen offset:1024\n"
    "s_lshl_b32 s78, s49, 1\n"
    "s_add_u32 s78, s78, s44\n"
    "buffer_load_dwordx4 a[224:227], %[l16], s[40:43], s78 offen\n"
    "buffer_load_dwordx4 a[228:231], %[l16], s[40:43], s78 offen offset:1024\n"
    "s_add_u32 s78, s78, 0x3800\n"
    "buffer_load_dwordx4 a[232:235], %[l16], s[40:43], s78 offen\n"
    "buffer_load_dwordx4 a[236:239], %[l16], s[40:43], s78 offen offset:1024\n"
    "s_add_u32 s78, s78, 0x3800\n"
    "buffer_load_dwordx4 a[240:243], %[l16], s[40:43], s78 offen\n"
    "buffer_load_dwordx4 a[244:247], %[l16], s[40:43], s78 offen offset:1024\n"
    "s_add_u32 s78, s78, 0x3800\n"
    "buffer_load_dwordx4 a[248:251], %[l16], s[40:43], s78 offen\n"
    "buffer_load_dwordx4 a[252:255], %[l16], s[40:43], s78 offen offset:1024\n"
    "s_add_u32 s78, s78, 0x3800\n"
    "buffer_load_dwordx4 v[120:123], %[l16], s[40:43], s78 offen\n"
    "buffer_load_dwordx4 v[124:127], %[l16], s[40:43], s78 offen offset:1024\n"
    "s_add_u32 s78, s78, 0x3800\n"
    "buffer_load_dwordx4 v[128:131], %[l16], s[40:43], s78 offen\n"
    "buffer_load_dwordx4 v[132:135], %[l16], s[40:43], s78 offen offset:1024\n"
    "s_add_u32 s78, s78, 0x3800\n"
    "buffer_load_dwordx4 v[136:139], %[l16], s[40:43], s78 offen\n"
    "buffer_load_dwordx4 v[140:143], %[l16], s[40:43], s78 offen offset:1024\n"
    "s_lshl_b32 s78, s50, 1\n"
    "s_add_u32 s78, s78, s44\n"
    "buffer_load_dwordx4 v[144:147], %[l16], s[40:43], s78 offen\n"
    "buffer_load_dwordx4 v[148:151], %[l16], s[40:43], s78 offen offset:1024\n"
    "s_add_u32 s78, s78, 0x3800\n"
    "buffer_load_dwordx4 v[152:155], %[l16], s[40:43], s78 offen\n"
    "buffer_load_dwordx4 v[156:159], %[l16], s[40:43], s78 offen offset:1024\n"
    "s_add_u32 s78, s78, 0x3800\n"
    "buffer_load_dwordx4 v[160:163], %[l16], s[40:43], s78 offen\n"
    "buffer_load_dwordx4 v[164:167], %[l16], s[40:43], s78 offen offset:1024\n"
    "s_add_u32 s78, s78, 0x3800\n"
    "buffer_load_dwordx4 v[168:171], %[l16], s[40:43], s78 offen\n"
    "buffer_load_dwordx4 v[172:175], %[l16], s[40:43], s78 offen offset:1024\n"
    "s_add_u32 s78, s78, 0x3800\n"
    "buffer_load_dwordx4 v[176:179], %[l16], s[40:43], s78 offen\n"
    "buffer_load_dwordx4 v[180:183], %[l16], s[40:43], s78 offen offset:1024\n"
    "s_add_u32 s78, s78, 0x3800\n"
    "buffer_load_dwordx4 v[184:187], %[l16], s[40:43], s78 offen\n"
    "buffer_load_dwordx4 v[188:191], %[l16], s[40:43], s78 offen offset:1024\n"
    "s_add_u32 s78, s78, 0x3800\n"
    "buffer_load_dwordx4 v[192:195], %[l16], s[40:43], s78 offen\n"
    "buffer_load_dwordx4 v[196:199], %[l16], s[40:43], s78 offen offset:1024\n"
    "s_lshl_b32 s78, s51, 1\n"
    "s_add_u32 s78, s78, s44\n"
    "buffer_load_dwordx4 v[200:203], %[l16], s[40:43], s78 offen\n"
    "buffer_load_dwordx4 v[204:207], %[l16], s[40:43], s78 offen offset:1024\n"
    "s_add_u32 s78, s78, 0x3800\n"
    "buffer_load_dwordx4 v[208:211], %[l16], s[40:43], s78 offen\n"
    "buffer_load_dwordx4 v[212:215], %[l16], s[40:43], s78 offen offset:1024\n"
    "s_add_u32 s78, s78, 0x3800\n"
    "buffer_load_dwordx4 v[216:219], %[l16], s[40:43], s78 offen\n"
    "buffer_load_dwordx4 v[220:223], %[l16], s[40:43], s78 offen offset:1024\n"
    "s_add_u32 s78, s78, 0x3800\n"
    "buffer_load_dwordx4 v[224:227], %[l16], s[40:43], s78 offen\n"
    "buffer_load_dwordx4 v[228:231], %[l16], s[40:43], s78 offen offset:1024\n"
    "s_add_u32 s78, s78, 0x3800\n"
    "buffer_load_dwordx4 v[232:235], %[l16], s[40:43], s78 offen\n"
    "buffer_load_dwordx4 v[236:239], %[l16], s[40:43], s78 offen offset:1024\n"
    "s_add_u32 s78, s78, 0x3800\n"
    "buffer_load_dwordx4 v[240:243], %[l16], s[40:43], s78 offen\n"
    "buffer_load_dwordx4 v[244:247], %[l16], s[40:43], s78 offen offset:1024\n"
    "s_add_u32 s78, s78, 0x3800\n"
    "buffer_load_dwordx4 v[248:251], %[l16], s[40:43], s78 offen\n"
    "buffer_load_dwordx4 v[252:255], %[l16], s[40:43], s78 offen offset:1024\n"
    "s_mov_b32 s78, 0x39de000\n"
    "s_mov_b32 s74, 0\n"
    "v_mov_b32 v20, 0\n"
    "sel_p_%=:\n"
    "buffer_load_dword v21, v20, s[40:43], s78 offen sc1\n"
    "s_waitcnt vmcnt(0)\n"
    "v_readfirstlane_b32 s99, v21\n"
    "s_nop 3\n"
    "s_cmp_lg_u32 s99, 0\n"
    "s_cbranch_scc1 sel_got_%=\n"
    "s_add_u32 s74, s74, 1\n"
    "s_sleep 2\n"
    "s_cmp_lt_u32 s74, s76\n"
    "s_cbranch_scc1 sel_p_%=\n"
    "s_mov_b32 s76, 1\n"
    "s_mov_b32 s99, 1\n"
    "sel_got_%=:\n"
    "s_sub_u32 s99, s99, 1\n"
    "s_and_b32 s99, s99, 3\n"
    "s_lshl_b32 s78, s99, 13\n"
    "s_add_u32 s59, s78, 0\n"
    "s_add_u32 s62, s59, %[pbase]\n"
    "s_add_u32 s60, s78, 0x8000\n"
    "s_add_u32 s63, s60, %[pbase]\n"
    "s_add_u32 s63, s63, 0x8000\n"
    "s_add_u32 s61, s78, 0x10000\n"
    "s_add_u32 s64, s61, %[pbase]\n"
    "s_add_u32 s64, s64, 0x10000\n"
    "v_mov_b32 v52, 0\n"
    "v_mov_b32 v53, 0\n"
    "v_mov_b32 v54, 0\n"
    "v_mov_b32 v55, 0\n"
    "v_mov_b32 v56, 0\n"
    "v_mov_b32 v57, 0\n"
    "v_mov_b32 v58, 0\n"
    "v_mov_b32 v59, 0\n"
    "v_mov_b32 v60, 0\n"
    "v_mov_b32 v61, 0\n"
    "v_mov_b32 v62, 0\n"
    "v_mov_b32 v63, 0\n"
    "v_mov_b32 v64, 0\n"
    "v_mov_b32 v65, 0\n"
    "v_mov_b32 v66, 0\n"
    "v_mov_b32 v67, 0\n"
    "v_mov_b32 v68, 0\n"
    "v_mov_b32 v69, 0\n"
    "v_mov_b32 v70, 0\n"
    "v_mov_b32 v71, 0\n"
    "v_mov_b32 v72, 0\n"
    "v_mov_b32 v73, 0\n"
    "v_mov_b32 v74, 0\n"
    "v_mov_b32 v75, 0\n"
    "v_mov_b32 v76, 0\n"
    "v_mov_b32 v77, 0\n"
    "v_mov_b32 v78, 0\n"
    "v_mov_b32 v79, 0\n"
    "v_add_u32 v32, s54, %[l16]\n"
    "v_add_u32 v36, s55, %[l16]\n"
    "v_add_u32 v40, s56, %[l16]\n"
    "v_add_u32 v44, s57, %[l16]\n"
    "v_add_u32 v48, s58, %[l16]\n"
    "s_add_u32 s65, s59, s45\n"
    "s_add_u32 s66, s59, s46\n"
    "s_waitcnt vmcnt(0)\n"
    "s_mov_b32 s30, 0x5dc\n"
    "s_mov_b32 s31, 0x258\n"
    "s_mov_b32 s36, 0\n"
    "s_mov_b32 s37, 0\n"
    "s_mov_b32 s39, 0\n"
    "s_memtime s[34:35]\n"
    "s_waitcnt lgkmcnt(0)\n"
    "s_mov_b32 s28, s34\n"
    "sc_loop_%=:\n"
    "scm_tw_%=:\n"
    "s_memtime s[34:35]\n"
    "s_waitcnt lgkmcnt(0)\n"
    "s_sub_u32 s73, s34, s28\n"
    "s_cmp_lt_i32 s73, 0xffffffec\n"
    "s_cbranch_scc1 scm_tw_%=\n"
    "s_mov_b32 s29, s34\n"
    "s_mov_b32 s74, 0\n"
    "scm_p_%=:\n"
    "buffer_load_dwordx4 v[24:27], %[l16], s[40:43], s65 offen sc1\n"
    "buffer_load_dwordx4 v[28:31], %[l16], s[40:43], s66 offen sc1\n"
    "s_waitcnt vmcnt(0)\n"
    "v_max3_u32 v108, v24, v25, v26\n"
    "v_max3_u32 v109, v27, v28, v29\n"
    "v_max3_u32 v108, v108, v30, v31\n"
    "v_max_u32 v108, v108, v109\n"
    "v_cmp_eq_u32 vcc, -1, v108\n"
    "ds_write_b128 %[mw0], v[24:27]\n"
    "ds_write_b128 %[mw1], v[28:31]\n"
    "s_cbranch_vccz scm_got_%=\n"
    "s_add_u32 s74, s74, 1\n"
    "s_cmp_lt_u32 s74, s76\n"
    "s_cbranch_scc1 scm_p_%=\n"
    "s_mov_b32 s76, 1\n"
    "scm_got_%=:\n"
    "s_memtime s[34:35]\n"
    "s_mov_b32 s83, s74\n"
    "v_smfmac_f32_16x16x64_bf16 v[52:55], v[24:27], a[0:7], %[idx]\n"
    "v_smfmac_f32_16x16x64_bf16 v[56:59], v[24:27], a[8:15], %[idx]\n"
    "v_smfmac_f32_16x16x64_bf16 v[60:63], v[24:27], a[16:23], %[idx]\n"
    "v_smfmac_f32_16x16x64_bf16 v[64:67], v[24:27], a[24:31], %[idx]\n"
    "v_smfmac_f32_16x16x64_bf16 v[68:71], v[24:27], a[32:39], %[idx]\n"
    "v_smfmac_f32_16x16x64_bf16 v[72:75], v[24:27], a[40:47], %[idx]\n"
    "v_smfmac_f32_16x16x64_bf16 v[76:79], v[24:27], a[48:55], %[idx]\n"
    "s_waitcnt lgkmcnt(0)\n"
    "s_barrier\n"
    "s_sub_u32 s36, s34, s29\n"
    "s_min_u32 s36, s36, 0xffff\n"
    "s_lshl_b32 s80, s74, 16\n"
    "s_or_b32 s36, s36, s80\n"
    "ds_read_b128 v[32:35], v32\n"
    "ds_read_b128 v[36:39], v36\n"
    "v_smfmac_f32_16x16x64_bf16 v[52:55], v[28:31], a[56:63], %[idx]\n"
    "ds_read_b128 v[40:43], v40\n"
    "ds_read_b128 v[44:47], v44\n"
    "v_smfmac_f32_16x16x64_bf16 v[56:59], v[28:31], a[64:71], %[idx]\n"
    "ds_read_b128 v[48:51], v48\n"
    "v_smfmac_f32_16x16x64_bf16 v[60:63], v[28:31], a[72:79], %[idx]\n"
    "v_smfmac_f32_16x16x64_bf16 v[64:67], v[28:31], a[80:87], %[idx]\n"
    "v_smfmac_f32_16x16x64_bf16 v[68:71], v[28:31], a[88:95], %[idx]\n"
    "v_smfmac_f32_16x16x64_bf16 v[72:75], v[28:31], a[96:103], %[idx]\n"
    "v_smfmac_f32_16x16x64_bf16 v[76:79], v[28:31], a[104:111], %[idx]\n"
    "s_waitcnt lgkmcnt(4)\n"
    "v_smfmac_f32_16x16x64_bf16 v[52:55], v[32:35], a[112:119], %[idx]\n"
    "v_smfmac_f32_16x16x64_bf16 v[56:59], v[32:35], a[120:127], %[idx]\n"
    "s_waitcnt lgkmcnt(3)\n"
    "v_smfmac_f32_16x16x64_bf16 v[52:55], v[36:39], a[168:175], %[idx]\n"
    "v_smfmac_f32_16x16x64_bf16 v[56:59], v[36:39], a[176:183], %[idx]\n"
    "s_waitcnt lgkmcnt(2)\n"
    "v_smfmac_f32_16x16x64_bf16 v[52:55], v[40:43], a[224:231], %[idx]\n"
    "v_smfmac_f32_16x16x64_bf16 v[56:59], v[40:43], a[232:239], %[idx]\n"
    "s_waitcnt lgkmcnt(1)\n"
    "v_smfmac_f32_16x16x64_bf16 v[52:55], v[44:47], v[144:151], %[idx]\n"
    "v_smfmac_f32_16x16x64_bf16 v[56:59], v[44:47], v[152:159], %[idx]\n"
    "s_waitcnt lgkmcnt(0)\n"
    "v_smfmac_f32_16x16x64_bf16 v[52:55], v[48:51], v[200:207], %[idx]\n"
    "v_smfmac_f32_16x16x64_bf16 v[56:59], v[48:51], v[208:215], %[idx]\n"
    "v_smfmac_f32_16x16x64_bf16 v[60:63], v[32:35], a[128:135], %[idx]\n"
    "v_smfmac_f32_16x16x64_bf16 v[64:67], v[32:35], a[136:143], %[idx]\n"
    "v_smfmac_f32_16x16x64_bf16 v[60:63], v[36:39], a[184:191], %[idx]\n"
    "v_smfmac_f32_16x16x64_bf16 v[64:67], v[36:39], a[192:199], %[idx]\n"
    "v_smfmac_f32_16x16x64_bf16 v[60:63], v[40:43], a[240:247], %[idx]\n"
    "v_smfmac_f32_16x16x64_bf16 v[64:67], v[40:43], a[248:255], %[idx]\n"
    "v_smfmac_f32_16x16x64_bf16 v[60:63], v[44:47], v[160:167], %[idx]\n"
    "v_cvt_pk_bf16_f32 v52, v52, v53\n"
    "v_cvt_pk_bf16_f32 v53, v54, v55\n"
    "v_smfmac_f32_16x16x64_bf16 v[64:67], v[44:47], v[168:175], %[idx]\n"
    "v_cvt_pk_bf16_f32 v56, v56, v57\n"
    "v_smfmac_f32_16x16x64_bf16 v[60:63], v[48:51], v[216:223], %[idx]\n"
    "v_cvt_pk_bf16_f32 v57, v58, v59\n"
    "ds_write_b64 %[tw], v[52:53]\n"
    "v_smfmac_f32_16x16x64_bf16 v[64:67], v[48:51], v[224:231], %[idx]\n"
    "ds_write_b64 %[tw], v[56:57] offset:512\n"
    "v_smfmac_f32_16x16x64_bf16 v[68:71], v[32:35], a[144:151], %[idx]\n"
    "ds_read_b64_tr_b16 v[52:53], %[tr1]\n"
    "v_smfmac_f32_16x16x64_bf16 v[72:75], v[32:35], a[152:159], %[idx]\n"
    "ds_read_b64_tr_b16 v[54:55], %[tr2]\n"
    "v_smfmac_f32_16x16x64_bf16 v[68:71], v[36:39], a[200:207], %[idx]\n"
    "v_smfmac_f32_16x16x64_bf16 v[72:75], v[36:39], a[208:215], %[idx]\n"
    "ds_read_b128 v[24:27], %[km]\n"
    "v_smfmac_f32_16x16x64_bf16 v[68:71], v[40:43], v[120:127], %[idx]\n"
    "ds_read_b128 v[28:31], %[km] offset:512\n"
    "v_smfmac_f32_16x16x64_bf16 v[72:75], v[40:43], v[128:135], %[idx]\n"
    "v_smfmac_f32_16x16x64_bf16 v[68:71], v[44:47], v[176:183], %[idx]\n"
    "v_cvt_pk_bf16_f32 v60, v60, v61\n"
    "v_cvt_pk_bf16_f32 v61, v62, v63\n"
    "v_smfmac_f32_16x16x64_bf16 v[72:75], v[44:47], v[184:191], %[idx]\n"
    "v_cvt_pk_bf16_f32 v64, v64, v65\n"
    "v_smfmac_f32_16x16x64_bf16 v[68:71], v[48:51], v[232:239], %[idx]\n"
    "v_cvt_pk_bf16_f32 v65, v66, v67\n"
    "ds_write_b64 %[tw], v[60:61] offset:1024\n"
    "v_smfmac_f32_16x16x64_bf16 v[72:75], v[48:51], v[240:247], %[idx]\n"
    "ds_write_b64 %[tw], v[64:65] offset:1536\n"
    "v_smfmac_f32_16x16x64_bf16 v[76:79], v[32:35], a[160:167], %[idx]\n"
    "ds_read_b64_tr_b16 v[60:61], %[tr1] offset:1024\n"
    "ds_read_b64_tr_b16 v[62:63], %[tr2] offset:1024\n"
    "v_smfmac_f32_16x16x64_bf16 v[76:79], v[36:39], a[216:223], %[idx]\n"
    "ds_read_b128 v[32:35], %[km] offset:1024\n"
    "v_smfmac_f32_16x16x64_bf16 v[76:79], v[40:43], v[136:143], %[idx]\n"
    "ds_read_b64 v[36:37], %[kmt]\n"
    "v_smfmac_f32_16x16x64_bf16 v[76:79], v[44:47], v[192:199], %[idx]\n"
    "v_smfmac_f32_16x16x64_bf16 v[76:79], v[48:51], v[248:255], %[idx]\n"
    "v_cvt_pk_bf16_f32 v68, v68, v69\n"
    "s_waitcnt lgkmcnt(7)\n"
    "v_mfma_f32_16x16x32_bf16 v[108:111], v[24:27], v[52:55], 0\n"
    "v_cvt_pk_bf16_f32 v69, v70, v71\n"
    "v_cvt_pk_bf16_f32 v72, v72, v73\n"
    "v_mfma_f32_16x16x32_bf16 v[112:115], v[80:83], v[52:55], 0\n"
    "v_cvt_pk_bf16_f32 v73, v74, v75\n"
    "ds_write_b64 %[tw], v[68:69] offset:2048\n"
    "v_mfma_f32_16x16x32_bf16 v[116:119], v[84:87], v[52:55], 0\n"
    "ds_write_b64 %[tw], v[72:73] offset:2560\n"
    "ds_read_b64_tr_b16 v[68:69], %[tr1] offset:2048\n"
    "ds_read_b64_tr_b16 v[70:71], %[tr2] offset:2048\n"
    "s_waitcnt lgkmcnt(6)\n"
    "v_mfma_f32_16x16x32_bf16 v[108:111], v[28:31], v[60:63], v[108:111]\n"
    "v_cvt_pk_bf16_f32 v76, v76, v77\n"
    "v_cvt_pk_bf16_f32 v77, v78, v79\n"
    "v_mfma_f32_16x16x32_bf16 v[112:115], v[88:91], v[60:63], v[112:115]\n"
    "ds_write_b64 %[tw], v[76:77] offset:3072\n"
    "ds_read_b64_tr_b16 v[78:79], %[trt]\n"
    "v_mfma_f32_16x16x32_bf16 v[116:119], v[92:95], v[60:63], v[116:119]\n"
    "s_nop 0\n"
    "s_waitcnt lgkmcnt(2)\n"
    "v_mfma_f32_16x16x32_bf16 v[108:111], v[32:35], v[68:71], v[108:111]\n"
    "v_mfma_f32_16x16x32_bf16 v[112:115], v[96:99], v[68:71], v[112:115]\n"
    "v_mfma_f32_16x16x32_bf16 v[116:119], v[100:103], v[68:71], v[116:119]\n"
    "s_waitcnt lgkmcnt(0)\n"
    "v_mfma_f32_16x16x16_bf16 v[108:111], v[36:37], v[78:79], v[108:111]\n"
    "v_mfma_f32_16x16x16_bf16 v[112:115], v[104:105], v[78:79], v[112:115]\n"
    "v_mfma_f32_16x16x16_bf16 v[116:119], v[106:107], v[78:79], v[116:119]\n"
    "s_nop 5\n"
    "v_add_f32_dpp v108, v108, v108 row_ror:8 row_mask:0xf bank_mask:0xf\n"
    "v_add_f32_dpp v109, v109, v109 row_ror:8 row_mask:0xf bank_mask:0xf\n"
    "v_add_f32_dpp v110, v110, v110 row_ror:8 row_mask:0xf bank_mask:0xf\n"
    "v_add_f32_dpp v111, v111, v111 row_ror:8 row_mask:0xf bank_mask:0xf\n"
    "ds_write_b128 %[sw], v[108:111]\n"
    "v_add_f32_dpp v112, v112, v112 row_ror:8 row_mask:0xf bank_mask:0xf\n"
    "v_add_f32_dpp v113, v113, v113 row_ror:8 row_mask:0xf bank_mask:0xf\n"
    "v_add_f32_dpp v114, v114, v114 row_ror:8 row_mask:0xf bank_mask:0xf\n"
    "v_add_f32_dpp v115, v115, v115 row_ror:8 row_mask:0xf bank_mask:0xf\n"
    "ds_write_b128 %[sw], v[112:115] offset:576\n"
    "v_add_f32_dpp v116, v116, v116 row_ror:8 row_mask:0xf bank_mask:0xf\n"
    "v_add_f32_dpp v117, v117, v117 row_ror:8 row_mask:0xf bank_mask:0xf\n"
    "v_add_f32_dpp v118, v118, v118 row_ror:8 row_mask:0xf bank_mask:0xf\n"
    "v_add_f32_dpp v119, v119, v119 row_ror:8 row_mask:0xf bank_mask:0xf\n"
    "ds_write_b128 %[sw], v[116:119] offset:1152\n"
    "s_waitcnt lgkmcnt(0)\n"
    "s_barrier\n"
    "s_cmp_lg_u32 s82, 0\n"
    "s_cbranch_scc1 sc_skip_%=\n"
    "ds_read_b128 v[52:55], %[srx]\n"
    "ds_read_b128 v[56:59], %[srx] offset:1792\n"
    "ds_read2st64_b32 v[68:69], %[srm] offset0:0 offset1:7\n"
    "ds_read_b128 v[60:63], %[srx] offset:3584\n"
    "ds_read_b128 v[64:67], %[srx] offset:5376\n"
    "ds_read2st64_b32 v[70:71], %[srm] offset0:14 offset1:21\n"
    "v_mov_b32 v108, -1\n"
    "v_mov_b32 v109, -1\n"
    "v_mov_b32 v110, -1\n"
    "v_mov_b32 v111, -1\n"
    "s_waitcnt lgkmcnt(4)\n"
    "v_pk_add_f32 v[52:53], v[52:53], v[56:57]\n"
    "v_pk_add_f32 v[54:55], v[54:55], v[58:59]\n"
    "s_waitcnt lgkmcnt(3)\n"
    "v_add_f32 v68, v68, v69\n"
    "s_waitcnt lgkmcnt(1)\n"
    "v_pk_add_f32 v[60:61], v[60:61], v[64:65]\n"
    "v_pk_add_f32 v[62:63], v[62:63], v[66:67]\n"
    "s_waitcnt lgkmcnt(0)\n"
    "v_add_f32 v70, v70, v71\n"
    "v_pk_add_f32 v[52:53], v[52:53], v[60:61]\n"
    "v_pk_add_f32 v[54:55], v[54:55], v[62:63]\n"
    "v_add_f32 v68, v68, v70\n"
    "v_max3_f32 v72, v52, v53, v54\n"
    "v_max3_f32 v72, v72, v55, v68\n"
    "s_nop 1\n"
    "v_max_f32_dpp v72, v72, v72 quad_perm:[1,0,3,2] row_mask:0xf bank_mask:0xf\n"
    "s_nop 1\n"
    "v_max_f32_dpp v72, v72, v72 quad_perm:[2,3,0,1] row_mask:0xf bank_mask:0xf\n"
    "s_nop 1\n"
    "v_max_f32_dpp v72, v72, v72 row_half_mirror row_mask:0xf bank_mask:0xf\n"
    "v_mul_f32 v73, s77, v72\n"
    "v_fma_f32 v52, v52, s77, -v73\n"
    "v_fma_f32 v53, v53, s77, -v73\n"
    "v_fma_f32 v54, v54, s77, -v73\n"
    "v_fma_f32 v55, v55, s77, -v73\n"
    "v_fma_f32 v68, v68, s77, -v73\n"
    "v_exp_f32 v52, v52\n"
    "v_exp_f32 v53, v53\n"
    "v_exp_f32 v54, v54\n"
    "v_exp_f32 v55, v55\n"
    "v_exp_f32 v68, v68\n"
    "v_add_f32 v74, v52, v53\n"
    "v_add_f32 v75, v54, v55\n"
    "v_add_f32 v74, v74, v75\n"
    "v_add_f32 v74, v74, v68\n"
    "s_nop 1\n"
    "v_add_f32_dpp v74, v74, v74 quad_perm:[1,0,3,2] row_mask:0xf bank_mask:0xf\n"
    "s_nop 1\n"
    "v_add_f32_dpp v74, v74, v74 quad_perm:[2,3,0,1] row_mask:0xf bank_mask:0xf\n"
    "s_nop 1\n"
    "v_add_f32_dpp v74, v74, v74 row_half_mirror row_mask:0xf bank_mask:0xf\n"
    "v_rcp_f32 v76, v74\n"
    "s_nop 0\n"
    "v_mul_f32 v68, v68, v76\n"
    "v_mul_f32 v52, v52, v76\n"
    "v_mul_f32 v53, v53, v76\n"
    "v_mul_f32 v54, v54, v76\n"
    "v_mul_f32 v55, v55, v76\n"
    "v_mov_b32_dpp v75, v68 quad_perm:[1,0,3,2] row_mask:0xf bank_mask:0xf\n"
    "v_cvt_pk_bf16_f32 v76, v52, v53\n"
    "v_cvt_pk_bf16_f32 v77, v54, v55\n"
    "v_cvt_pk_bf16_f32 v68, v68, v75\n"
    "s_nop 0\n"
    "v_mov_b32_dpp v78, v76 quad_perm:[1,0,3,2] row_mask:0xf bank_mask:0xf\n"
    "v_mov_b32_dpp v79, v77 quad_perm:[1,0,3,2] row_mask:0xf bank_mask:0xf\n"
    "buffer_store_dwordx4 v[76:79], %[pox], s[40:43], s62 offen sc1\n"
    "v_mov_b32_dpp v69, v68 row_shl:2 row_mask:0xf bank_mask:0xf\n"
    "v_mov_b32_dpp v70, v68 row_shl:4 row_mask:0xf bank_mask:0xf\n"
    "v_mov_b32_dpp v71, v68 row_shl:6 row_mask:0xf bank_mask:0xf\n"
    "buffer_store_dwordx4 v[68:71], %[pom], s[40:43], s62 offen sc1\n"
    "buffer_store_dwordx4 v[108:111], %[pox], s[40:43], s64 offen sc1\n"
    "buffer_store_dwordx4 v[108:111], %[pom], s[40:43], s64 offen sc1\n"
    "s_nop 1\n"
    "sc_skip_%=:\n"
    "s_barrier\n"
    "s_memtime s[34:35]\n"
    "buffer_load_dwordx4 v[80:83], %[l16], s[40:43], s67 offen\n"
    "buffer_load_dwordx4 v[84:87], %[l16], s[40:43], s67 offen offset:1024\n"
    "buffer_load_dwordx4 v[88:91], %[l16], s[40:43], s67 offen offset:2048\n"
    "buffer_load_dwordx4 v[92:95], %[l16], s[40:43], s67 offen offset:3072\n"
    "s_add_u32 s79, s67, 0x1000\n"
    "buffer_load_dwordx4 v[96:99], %[l16], s[40:43], s79 offen\n"
    "buffer_load_dwordx4 v[100:103], %[l16], s[40:43], s79 offen offset:1024\n"
    "buffer_load_dwordx4 v[104:107], %[l16], s[40:43], s79 offen offset:2048\n"
    "s_add_u32 s67, s67, 0x7000\n"
    "s_cmp_eq_u32 s70, 0\n"
    "s_cbranch_scc1 sc_noupd_%=\n"
    "s_and_b32 s80, s36, 0xffff\n"
    "s_sub_u32 s80, s80, s31\n"
    "s_ashr_i32 s81, s80, 1\n"
    "s_min_i32 s81, s81, 0x80\n"
    "s_max_i32 s81, s81, 0xffffff80\n"
    "s_mov_b32 s71, -10\n"
    "s_cmp_gt_i32 s80, 50\n"
    "s_cselect_b32 s71, s81, s71\n"
    "s_cmp_lt_i32 s80, 0xffffffd8\n"
    "s_cselect_b32 s71, s81, s71\n"
    "s_cmp_gt_u32 s36, 0xffff\n"
    "s_cselect_b32 s71, 0x80, s71\n"
    "s_add_u32 s30, s30, s71\n"
    "s_max_i32 s30, s30, 0x2bc\n"
    "s_min_i32 s30, s30, 0x8fc\n"
    "sc_noupd_%=:\n"
    "s_waitcnt lgkmcnt(0)\n"
    "s_add_u32 s28, s34, s30\n"
    "s_mov_b32 s73, s59\n"
    "s_mov_b32 s59, s60\n"
    "s_mov_b32 s60, s61\n"
    "s_mov_b32 s61, s73\n"
    "s_mov_b32 s73, s62\n"
    "s_mov_b32 s62, s63\n"
    "s_mov_b32 s63, s64\n"
    "s_mov_b32 s64, s73\n"
    "v_mov_b32 v52, 0\n"
    "v_mov_b32 v53, 0\n"
    "v_mov_b32 v54, 0\n"
    "v_mov_b32 v55, 0\n"
    "v_mov_b32 v56, 0\n"
    "v_mov_b32 v57, 0\n"
    "v_mov_b32 v58, 0\n"
    "v_mov_b32 v59, 0\n"
    "v_mov_b32 v60, 0\n"
    "v_mov_b32 v61, 0\n"
    "v_mov_b32 v62, 0\n"
    "v_mov_b32 v63, 0\n"
    "v_mov_b32 v64, 0\n"
    "v_mov_b32 v65, 0\n"
    "v_mov_b32 v66, 0\n"
    "v_mov_b32 v67, 0\n"
    "v_mov_b32 v68, 0\n"
    "v_mov_b32 v69, 0\n"
    "v_mov_b32 v70, 0\n"
    "v_mov_b32 v71, 0\n"
    "v_mov_b32 v72, 0\n"
    "v_mov_b32 v73, 0\n"
    "v_mov_b32 v74, 0\n"
    "v_mov_b32 v75, 0\n"
    "v_mov_b32 v76, 0\n"
    "v_mov_b32 v77, 0\n"
    "v_mov_b32 v78, 0\n"
    "v_mov_b32 v79, 0\n"
    "v_add_u32 v32, s54, %[l16]\n"
    "v_add_u32 v36, s55, %[l16]\n"
    "v_add_u32 v40, s56, %[l16]\n"
    "v_add_u32 v44, s57, %[l16]\n"
    "v_add_u32 v48, s58, %[l16]\n"
    "s_add_u32 s65, s59, s45\n"
    "s_add_u32 s66, s59, s46\n"
    "s_add_u32 s70, s70, 1\n"
    "s_cmp_lt_u32 s70, 63\n"
    "s_cbranch_scc1 sc_loop_%=\n"
    "s_waitcnt vmcnt(0) lgkmcnt(0)\n"
        :
        : [wslo] "s"(wslo), [wshi] "s"(wshi), [wsend] "s"(WS_SIZE), [wbase] "s"(AIMG_OFF + (h * 28 + 7 * w) * 14 * 1024), [kbpack] "s"(pk),
          [ldsbase] "s"(lds), [xk] "s"(XK_OFF + w * 7168), [upbase] "s"(UIMG_OFF + h * 28672), [pbase] "s"((3 + (grp >> 1)) * 32768 + (grp & 1) * 2560),
          [dbg] "s"(lds + 81920 + w * 4096), [scale] "s"(__float_as_uint(0.0625f * 1.4426950408889634f)), [wv] "s"(w),
          [l16] "v"(lane * 16), [idx] "v"((lane & 8) ? 0xEEEE : 0x4444), [mw0] "v"(lds + (pk & 15) * 1024 + lane * 16),
          [mw1] "v"(lds + ((pk >> 4) & 15) * 1024 + lane * 16), [tw] "v"(Tb + taddr(r16, g)), [tr1] "v"(tr1), [tr2] "v"(tr1 ^ 0x88),
          [trt] "v"(Tb + taddr(96 + 4 * g + (r16 >> 2), r16 & 3)), [km] "v"(lds + (14 * w + g) * 128 + (r16 & 7) * 16),
          [kmt] "v"(lds + (14 * w + 12 + (g >> 1)) * 128 + (r16 & 7) * 16 + (g & 1) * 8), [upf] "v"(lane < 56 ? (w * 56 + lane) * 128 : 0x7FFFFFF0),
          [sw] "v"(Sb + w * 1792 + g * 144 + (r16 & 7) * 16),
          [srx] "v"(Sb + (1 + (c >> 2)) * 576 + (c & 3) * 144 + i * 16), [srm] "v"(Sb + (c >> 2) * 144 + i * 16 + (c & 3) * 4),
          [pox] "v"((c & 1) ? 0x7FFFFFF0 : 512 * (1 + hh) + 128 * (c >> 1) + 16 * i), [pom] "v"(c ? 0x7FFFFFF0 : 128 * hh + 16 * i),
          [tsw] "v"(w == 1 && lane < (0 && 1 ? 8 : 1) ? (0 ? TS2_OFF : FLAG_OFF + 8192) + h * 128 + lane * 16 : 0x7FFFFFF0)
        : "v20", "v21", "v22", "v23", "v24", "v25", "v26", "v27", "v28", "v29", "v30", "v31", "v32", "v33", "v34", "v35", "v36", "v37", "v38", "v39",
          "v40", "v41", "v42", "v43", "v44", "v45", "v46", "v47", "v48", "v49", "v50", "v51", "v52", "v53", "v54", "v55", "v56", "v57", "v58", "v59",
          "v60", "v61", "v62", "v63", "v64", "v65", "v66", "v67", "v68", "v69", "v70", "v71", "v72", "v73", "v74", "v75", "v76", "v77", "v78", "v79",
          "v80", "v81", "v82", "v83", "v84", "v85", "v86", "v87", "v88", "v89", "v90", "v91", "v92", "v93", "v94", "v95", "v96", "v97", "v98", "v99",
          "v100", "v101", "v102", "v103", "v104", "v105", "v106", "v107", "v108", "v109", "v110", "v111", "v112", "v113", "v114", "v115", "v116",
          "v117", "v118", "v119", "v120", "v121", "v122", "v123", "v124", "v125", "v126", "v127", "v128", "v129", "v130", "v131", "v132", "v133",
          "v134", "v135", "v136", "v137", "v138", "v139", "v140", "v141", "v142", "v143", "v144", "v145", "v146", "v147", "v148", "v149", "v150",
          "v151", "v152", "v153", "v154", "v155", "v156", "v157", "v158", "v159", "v160", "v161", "v162", "v163", "v164", "v165", "v166", "v167",
          "v168", "v169", "v170", "v171", "v172", "v173", "v174", "v175", "v176", "v177", "v178", "v179", "v180", "v181", "v182", "v183", "v184",
          "v185", "v186", "v187", "v188", "v189", "v190", "v191", "v192", "v193", "v194", "v195", "v196", "v197", "v198", "v199", "v200", "v201",
          "v202", "v203", "v204", "v205", "v206", "v207", "v208", "v209", "v210", "v211", "v212", "v213", "v214", "v215", "v216", "v217", "v218",
          "v219", "v220", "v221", "v222", "v223", "v224", "v225", "v226", "v227", "v228", "v229", "v230", "v231", "v232", "v233", "v234", "v235",
          "v236", "v237", "v238", "v239", "v240", "v241", "v242", "v243", "v244", "v245", "v246", "v247", "v248", "v249", "v250", "v251", "v252",
          "v253", "v254", "v255", "a0", "a1", "a2", "a3", "a4", "a5", "a6", "a7", "a8", "a9", "a10", "a11", "a12", "a13", "a14", "a15", "a16", "a17",
          "a18", "a19", "a20", "a21", "a22", "a23", "a24", "a25", "a26", "a27", "a28", "a29", "a30", "a31", "a32", "a33", "a34", "a35", "a36", "a37",
          "a38", "a39", "a40", "a41", "a42", "a43", "a44", "a45", "a46", "a47", "a48", "a49", "a50", "a51", "a52", "a53", "a54", "a55", "a56", "a57",
          "a58", "a59", "a60", "a61", "a62", "a63", "a64", "a65", "a66", "a67", "a68", "a69", "a70", "a71", "a72", "a73", "a74", "a75", "a76", "a77",
          "a78", "a79", "a80", "a81", "a82", "a83", "a84", "a85", "a86", "a87", "a88", "a89", "a90", "a91", "a92", "a93", "a94", "a95", "a96", "a97",
          "a98", "a99", "a100", "a101", "a102", "a103", "a104", "a105", "a106", "a107", "a108", "a109", "a110", "a111", "a112", "a113", "a114", "a115",
          "a116", "a117", "a118", "a119", "a120", "a121", "a122", "a123", "a124", "a125", "a126", "a127", "a128", "a129", "a130", "a131", "a132",
          "a133", "a134", "a135", "a136", "a137", "a138", "a139", "a140", "a141", "a142", "a143", "a144", "a145", "a146", "a147", "a148", "a149",
          "a150", "a151", "a152", "a153", "a154", "a155", "a156", "a157", "a158", "a159", "a160", "a161", "a162", "a163", "a164", "a165", "a166",
          "a167", "a168", "a169", "a170", "a171", "a172", "a173", "a174", "a175", "a176", "a177", "a178", "a179", "a180", "a181", "a182", "a183",
          "a184", "a185", "a186", "a187", "a188", "a189", "a190", "a191", "a192", "a193", "a194", "a195", "a196", "a197", "a198", "a199", "a200",
          "a201", "a202", "a203", "a204", "a205", "a206", "a207", "a208", "a209", "a210", "a211", "a212", "a213", "a214", "a215", "a216", "a217",
          "a218", "a219", "a220", "a221", "a222", "a223", "a224", "a225", "a226", "a227", "a228", "a229", "a230", "a231", "a232", "a233", "a234",
          "a235", "a236", "a237", "a238", "a239", "a240", "a241", "a242", "a243", "a244", "a245", "a246", "a247", "a248", "a249", "a250", "a251",
          "a252", "a253", "a254", "a255", "s28", "s29", "s30", "s31", "s34", "s35", "s36", "s37", "s38", "s39", "s40", "s41", "s42", "s43", "s44",
          "s45", "s46", "s47", "s48", "s49", "s50", "s51", "s52", "s53", "s54", "s55", "s56", "s57", "s58", "s59", "s60", "s61", "s62", "s63", "s64",
          "s65", "s66", "s67", "s68", "s69", "s70", "s71", "s72", "s73", "s74", "s75", "s76", "s77", "s78", "s79", "s80", "s81", "s82", "s83", "s84",
          "s85", "s86", "s87", "s88", "s89", "s90", "s91", "s92", "s93", "s94", "s95", "s96", "s97", "s98", "s99", "vcc", "scc", "memory");
}

DI void value_cu(const Params& p, rsrc_t ws, int v, int w, int lane, char* smem) {
    const unsigned lds = (unsigned)(uintptr_t)(LAS char*)smem;
    const unsigned red = lds + 8192;
    char* Wl = smem + 16384 + w * 16384;
    const int g = lane >> 4, r16 = lane & 15, mi = lane >> 3, mc = lane & 7;
    const int pk = kb_pack(w);
    const int kb6 = (pk >> 24) & 15;
#pragma unroll
    for (int j = 0; j < 8; j++) {
        const int so = BIMG_OFF + (((4 * w + (j >> 1)) * 28 + 2 * v + (j & 1)) * 14 + 2 * kb6) * 1024;
        *(LAS u32x4*)(Wl + j * 2048 + lane * 16) = bl128<0>(ws, lane * 16, so);
        *(LAS u32x4*)(Wl + j * 2048 + 1024 + lane * 16) = bl128<0>(ws, lane * 16, so + 1024);
    }
    const unsigned wslo = (unsigned)(uintptr_t)p.ws, wshi = (unsigned)((uintptr_t)p.ws >> 32);
    asm volatile(
    "s_mov_b32 s40, %[wslo]\n"
    "s_and_b32 s41, %[wshi], 0xffff\n"
    "s_mov_b32 s42, %[wsend]\n"
    "s_mov_b32 s43, 0x20000\n"
    "s_mov_b32 s44, %[wbase]\n"
    "s_bfe_u32 s45, %[kbpack], 0x40000\n"
    "s_lshl_b32 s45, s45, 10\n"
    "s_add_u32 s52, s45, %[ldsbase]\n"
    "s_bfe_u32 s46, %[kbpack], 0x40004\n"
    "s_lshl_b32 s46, s46, 10\n"
    "s_add_u32 s53, s46, %[ldsbase]\n"
    "s_bfe_u32 s47, %[kbpack], 0x40008\n"
    "s_lshl_b32 s47, s47, 10\n"
    "s_add_u32 s54, s47, %[ldsbase]\n"
    "s_bfe_u32 s48, %[kbpack], 0x4000c\n"
    "s_lshl_b32 s48, s48, 10\n"
    "s_add_u32 s55, s48, %[ldsbase]\n"
    "s_bfe_u32 s49, %[kbpack], 0x40010\n"
    "s_lshl_b32 s49, s49, 10\n"
    "s_add_u32 s56, s49, %[ldsbase]\n"
    "s_bfe_u32 s50, %[kbpack], 0x40014\n"
    "s_lshl_b32 s50, s50, 10\n"
    "s_add_u32 s57, s50, %[ldsbase]\n"
    "s_bfe_u32 s51, %[kbpack], 0x40018\n"
    "s_lshl_b32 s51, s51, 10\n"
    "s_add_u32 s58, s51, %[ldsbase]\n"
    "s_mov_b32 s70, 0\n"
    "s_mov_b32 s76, 0x4000\n"
    "s_mov_b32 s67, %[ubase]\n"
    "s_mov_b32 s68, %[vmbase]\n"
    "s_mov_b32 s69, %[mhbase]\n"
    "s_mov_b32 s72, 0\n"
    "s_mov_b32 s83, 0\n"
    "s_mov_b32 s96, %[dbg]\n"
    "s_mov_b32 s97, %[uflag]\n"
    "s_mov_b32 s98, 0\n"
    "s_lshl_b32 s78, s45, 1\n"
    "s_add_u32 s78, s78, s44\n"
    "s_add_u32 s79, s78, 0\n"
    "buffer_load_dwordx4 a[0:3], %[l16], s[40:43], s79 offen\n"
    "buffer_load_dwordx4 a[4:7], %[l16], s[40:43], s79 offen offset:1024\n"
    "s_add_u32 s79, s78, 0x3800\n"
    "buffer_load_dwordx4 a[8:11], %[l16], s[40:43], s79 offen\n"
    "buffer_load_dwordx4 a[12:15], %[l16], s[40:43], s79 offen offset:1024\n"
    "s_add_u32 s79, s78, 0x62000\n"
    "buffer_load_dwordx4 a[16:19], %[l16], s[40:43], s79 offen\n"
    "buffer_load_dwordx4 a[20:23], %[l16], s[40:43], s79 offen offset:1024\n"
    "s_add_u32 s79, s78, 0x65800\n"
    "buffer_load_dwordx4 a[24:27], %[l16], s[40:43], s79 offen\n"
    "buffer_load_dwordx4 a[28:31], %[l16], s[40:43], s79 offen offset:1024\n"
    "s_add_u32 s79, s78, 0xc4000\n"
    "buffer_load_dwordx4 a[32:35], %[l16], s[40:43], s79 offen\n"
    "buffer_load_dwordx4 a[36:39], %[l16], s[40:43], s79 offen offset:1024\n"
    "s_add_u32 s79, s78, 0xc7800\n"
    "buffer_load_dwordx4 a[40:43], %[l16], s[40:43], s79 offen\n"
    "buffer_load_dwordx4 a[44:47], %[l16], s[40:43], s79 offen offset:1024\n"
    "s_add_u32 s79, s78, 0x126000\n"
    "buffer_load_dwordx4 a[48:51], %[l16], s[40:43], s79 offen\n"
    "buffer_load_dwordx4 a[52:55], %[l16], s[40:43], s79 offen offset:1024\n"
    "s_add_u32 s79, s78, 0x129800\n"
    "buffer_load_dwordx4 a[56:59], %[l16], s[40:43], s79 offen\n"
    "buffer_load_dwordx4 a[60:63], %[l16], s[40:43], s79 offen offset:1024\n"
    "s_lshl_b32 s78, s46, 1\n"
    "s_add_u32 s78, s78, s44\n"
    "s_add_u32 s79, s78, 0\n"
    "buffer_load_dwordx4 a[64:67], %[l16], s[40:43], s79 offen\n"
    "buffer_load_dwordx4 a[68:71], %[l16], s[40:43], s79 offen offset:1024\n"
    "s_add_u32 s79, s78, 0x3800\n"
    "buffer_load_dwordx4 a[72:75], %[l16], s[40:43], s79 offen\n"
    "buffer_load_dwordx4 a[76:79], %[l16], s[40:43], s79 offen offset:1024\n"
    "s_add_u32 s79, s78, 0x62000\n"
    "buffer_load_dwordx4 a[80:83], %[l16], s[40:43], s79 offen\n"
    "buffer_load_dwordx4 a[84:87], %[l16], s[40:43], s79 offen offset:1024\n"
    "s_add_u32 s79, s78, 0x65800\n"
    "buffer_load_dwordx4 a[88:91], %[l16], s[40:43], s79 offen\n"
    "buffer_load_dwordx4 a[92:95], %[l16], s[40:43], s79 offen offset:1024\n"
    "s_add_u32 s79, s78, 0xc4000\n"
    "buffer_load_dwordx4 a[96:99], %[l16], s[40:43], s79 offen\n"
    "buffer_load_dwordx4 a[100:103], %[l16], s[40:43], s79 offen offset:1024\n"
    "s_add_u32 s79, s78, 0xc7800\n"
    "buffer_load_dwordx4 a[104:107], %[l16], s[40:43], s79 offen\n"
    "buffer_load_dwordx4 a[108:111], %[l16], s[40:43], s79 offen offset:1024\n"
    "s_add_u32 s79, s78, 0x126000\n"
    "buffer_load_dwordx4 a[112:115], %[l16], s[40:43], s79 offen\n"
    "buffer_load_dwordx4 a[116:119], %[l16], s[40:43], s79 offen offset:1024\n"
    "s_add_u32 s79, s78, 0x129800\n"
    "buffer_load_dwordx4 a[120:123], %[l16], s[40:43], s79 offen\n"
    "buffer_load_dwordx4 a[124:127], %[l16], s[40:43], s79 offen offset:1024\n"
    "s_lshl_b32 s78, s47, 1\n"
    "s_add_u32 s78, s78, s44\n"
    "s_add_u32 s79, s78, 0\n"
    "buffer_load_dwordx4 a[128:131], %[l16], s[40:43], s79 offen\n"
    "buffer_load_dwordx4 a[132:135], %[l16], s[40:43], s79 offen offset:1024\n"
    "s_add_u32 s79, s78, 0x3800\n"
    "buffer_load_dwordx4 a[136:139], %[l16], s[40:43], s79 offen\n"
    "buffer_load_dwordx4 a[140:143], %[l16], s[40:43], s79 offen offset:1024\n"
    "s_add_u32 s79, s78, 0x62000\n"
    "buffer_load_dwordx4 a[144:147], %[l16], s[40:43], s79 offen\n"
    "buffer_load_dwordx4 a[148:151], %[l16], s[40:43], s79 offen offset:1024\n"
    "s_add_u32 s79, s78, 0x65800\n"
    "buffer_load_dwordx4 a[152:155], %[l16], s[40:43], s79 offen\n"
    "buffer_load_dwordx4 a[156:159], %[l16], s[40:43], s79 offen offset:1024\n"
    "s_add_u32 s79, s78, 0xc4000\n"
    "buffer_load_dwordx4 a[160:163], %[l16], s[40:43], s79 offen\n"
    "buffer_load_dwordx4 a[164:167], %[l16], s[40:43], s79 offen offset:1024\n"
    "s_add_u32 s79, s78, 0xc7800\n"
    "buffer_load_dwordx4 a[168:171], %[l16], s[40:43], s79 offen\n"
    "buffer_load_dwordx4 a[172:175], %[l16], s[40:43], s79 offen offset:1024\n"
    "s_add_u32 s79, s78, 0x126000\n"
    "buffer_load_dwordx4 a[176:179], %[l16], s[40:43], s79 offen\n"
    "buffer_load_dwordx4 a[180:183], %[l16], s[40:43], s79 offen offset:1024\n"
    "s_add_u32 s79, s78, 0x129800\n"
    "buffer_load_dwordx4 a[184:187], %[l16], s[40:43], s79 offen\n"
    "buffer_load_dwordx4 a[188:191], %[l16], s[40:43], s79 offen offset:1024\n"
    "s_lshl_b32 s78, s48, 1\n"
    "s_add_u32 s78, s78, s44\n"
    "s_add_u32 s79, s78, 0\n"
    "buffer_load_dwordx4 a[192:195], %[l16], s[40:43], s79 offen\n"
    "buffer_load_dwordx4 a[196:199], %[l16], s[40:43], s79 offen offset:1024\n"
    "s_add_u32 s79, s78, 0x3800\n"
    "buffer_load_dwordx4 a[200:203], %[l16], s[40:43], s79 offen\n"
    "buffer_load_dwordx4 a[204:207], %[l16], s[40:43], s79 offen offset:1024\n"
    "s_add_u32 s79, s78, 0x62000\n"
    "buffer_load_dwordx4 a[208:211], %[l16], s[40:43], s79 offen\n"
    "buffer_load_dwordx4 a[212:215], %[l16], s[40:43], s79 offen offset:1024\n"
    "s_add_u32 s79, s78, 0x65800\n"
    "buffer_load_dwordx4 a[216:219], %[l16], s[40:43], s79 offen\n"
    "buffer_load_dwordx4 a[220:223], %[l16], s[40:43], s79 offen offset:1024\n"
    "s_add_u32 s79, s78, 0xc4000\n"
    "buffer_load_dwordx4 a[224:227], %[l16], s[40:43], s79 offen\n"
    "buffer_load_dwordx4 a[228:231], %[l16], s[40:43], s79 offen offset:1024\n"
    "s_add_u32 s79, s78, 0xc7800\n"
    "buffer_load_dwordx4 a[232:235], %[l16], s[40:43], s79 offen\n"
    "buffer_load_dwordx4 a[236:239], %[l16], s[40:43], s79 offen offset:1024\n"
    "s_add_u32 s79, s78, 0x126000\n"
    "buffer_load_dwordx4 a[240:243], %[l16], s[40:43], s79 offen\n"
    "buffer_load_dwordx4 a[244:247], %[l16], s[40:43], s79 offen offset:1024\n"
    "s_add_u32 s79, s78, 0x129800\n"
    "buffer_load_dwordx4 a[248:251], %[l16], s[40:43], s79 offen\n"
    "buffer_load_dwordx4 a[252:255], %[l16], s[40:43], s79 offen offset:1024\n"
    "s_lshl_b32 s78, s49, 1\n"
    "s_add_u32 s78, s78, s44\n"
    "s_add_u32 s79, s78, 0\n"
    "buffer_load_dwordx4 v[128:131], %[l16], s[40:43], s79 offen\n"
    "buffer_load_dwordx4 v[132:135], %[l16], s[40:43], s79 offen offset:1024\n"
    "s_add_u32 s79, s78, 0x3800\n"
    "buffer_load_dwordx4 v[136:139], %[l16], s[40:43], s79 offen\n"
    "buffer_load_dwordx4 v[140:143], %[l16], s[40:43], s79 offen offset:1024\n"
    "s_add_u32 s79, s78, 0x62000\n"
    "buffer_load_dwordx4 v[144:147], %[l16], s[40:43], s79 offen\n"
    "buffer_load_dwordx4 v[148:151], %[l16], s[40:43], s79 offen offset:1024\n"
    "s_add_u32 s79, s78, 0x65800\n"
    "buffer_load_dwordx4 v[152:155], %[l16], s[40:43], s79 offen\n"
    "buffer_load_dwordx4 v[156:159], %[l16], s[40:43], s79 offen offset:1024\n"
    "s_add_u32 s79, s78, 0xc4000\n"
    "buffer_load_dwordx4 v[160:163], %[l16], s[40:43], s79 offen\n"
    "buffer_load_dwordx4 v[164:167], %[l16], s[40:43], s79 offen offset:1024\n"
    "s_add_u32 s79, s78, 0xc7800\n"
    "buffer_load_dwordx4 v[168:171], %[l16], s[40:43], s79 offen\n"
    "buffer_load_dwordx4 v[172:175], %[l16], s[40:43], s79 offen offset:1024\n"
    "s_add_u32 s79, s78, 0x126000\n"
    "buffer_load_dwordx4 v[176:179], %[l16], s[40:43], s79 offen\n"
    "buffer_load_dwordx4 v[180:183], %[l16], s[40:43], s79 offen offset:1024\n"
    "s_add_u32 s79, s78, 0x129800\n"
    "buffer_load_dwordx4 v[184:187], %[l16], s[40:43], s79 offen\n"
    "buffer_load_dwordx4 v[188:191], %[l16], s[40:43], s79 offen offset:1024\n"
    "s_lshl_b32 s78, s50, 1\n"
    "s_add_u32 s78, s78, s44\n"
    "s_add_u32 s79, s78, 0\n"
    "buffer_load_dwordx4 v[192:195], %[l16], s[40:43], s79 offen\n"
    "buffer_load_dwordx4 v[196:199], %[l16], s[40:43], s79 offen offset:1024\n"
    "s_add_u32 s79, s78, 0x3800\n"
    "buffer_load_dwordx4 v[200:203], %[l16], s[40:43], s79 offen\n"
    "buffer_load_dwordx4 v[204:207], %[l16], s[40:43], s79 offen offset:1024\n"
    "s_add_u32 s79, s78, 0x62000\n"
    "buffer_load_dwordx4 v[208:211], %[l16], s[40:43], s79 offen\n"
    "buffer_load_dwordx4 v[212:215], %[l16], s[40:43], s79 offen offset:1024\n"
    "s_add_u32 s79, s78, 0x65800\n"
    "buffer_load_dwordx4 v[216:219], %[l16], s[40:43], s79 offen\n"
    "buffer_load_dwordx4 v[220:223], %[l16], s[40:43], s79 offen offset:1024\n"
    "s_add_u32 s79, s78, 0xc4000\n"
    "buffer_load_dwordx4 v[224:227], %[l16], s[40:43], s79 offen\n"
    "buffer_load_dwordx4 v[228:231], %[l16], s[40:43], s79 offen offset:1024\n"
    "s_add_u32 s79, s78, 0xc7800\n"
    "buffer_load_dwordx4 v[232:235], %[l16], s[40:43], s79 offen\n"
    "buffer_load_dwordx4 v[236:239], %[l16], s[40:43], s79 offen offset:1024\n"
    "s_add_u32 s79, s78, 0x126000\n"
    "buffer_load_dwordx4 v[240:243], %[l16], s[40:43], s79 offen\n"
    "buffer_load_dwordx4 v[244:247], %[l16], s[40:43], s79 offen offset:1024\n"
    "s_add_u32 s79, s78, 0x129800\n"
    "buffer_load_dwordx4 v[248:251], %[l16], s[40:43], s79 offen\n"
    "buffer_load_dwordx4 v[252:255], %[l16], s[40:43], s79 offen offset:1024\n"
    "buffer_load_dwordx4 v[76:79], %[moff], s[40:43], s69 offen\n"
    "s_mov_b32 s78, 0x39de000\n"
    "s_mov_b32 s74, 0\n"
    "v_mov_b32 v73, 0\n"
    "sel_p_%=:\n"
    "buffer_load_dword v72, v73, s[40:43], s78 offen sc1\n"
    "s_waitcnt vmcnt(0)\n"
    "v_readfirstlane_b32 s99, v72\n"
    "s_nop 3\n"
    "s_cmp_lg_u32 s99, 0\n"
    "s_cbranch_scc1 sel_got_%=\n"
    "s_add_u32 s74, s74, 1\n"
    "s_sleep 2\n"
    "s_cmp_lt_u32 s74, s76\n"
    "s_cbranch_scc1 sel_p_%=\n"
    "s_mov_b32 s76, 1\n"
    "s_mov_b32 s99, 1\n"
    "sel_got_%=:\n"
    "s_sub_u32 s99, s99, 1\n"
    "s_and_b32 s99, s99, 3\n"
    "s_lshl_b32 s78, s99, 13\n"
    "s_add_u32 s59, s78, 0\n"
    "s_add_u32 s62, s59, %[pbase]\n"
    "s_add_u32 s60, s78, 0x8000\n"
    "s_add_u32 s63, s60, %[pbase]\n"
    "s_add_u32 s63, s63, 0x8000\n"
    "s_add_u32 s61, s78, 0x10000\n"
    "s_add_u32 s64, s61, %[pbase]\n"
    "s_add_u32 s64, s64, 0x10000\n"
    "v_mov_b32 v40, 0\n"
    "v_mov_b32 v41, 0\n"
    "v_mov_b32 v42, 0\n"
    "v_mov_b32 v43, 0\n"
    "v_mov_b32 v44, 0\n"
    "v_mov_b32 v45, 0\n"
    "v_mov_b32 v46, 0\n"
    "v_mov_b32 v47, 0\n"
    "v_mov_b32 v48, 0\n"
    "v_mov_b32 v49, 0\n"
    "v_mov_b32 v50, 0\n"
    "v_mov_b32 v51, 0\n"
    "v_mov_b32 v52, 0\n"
    "v_mov_b32 v53, 0\n"
    "v_mov_b32 v54, 0\n"
    "v_mov_b32 v55, 0\n"
    "v_mov_b32 v56, 0\n"
    "v_mov_b32 v57, 0\n"
    "v_mov_b32 v58, 0\n"
    "v_mov_b32 v59, 0\n"
    "v_mov_b32 v60, 0\n"
    "v_mov_b32 v61, 0\n"
    "v_mov_b32 v62, 0\n"
    "v_mov_b32 v63, 0\n"
    "v_mov_b32 v64, 0\n"
    "v_mov_b32 v65, 0\n"
    "v_mov_b32 v66, 0\n"
    "v_mov_b32 v67, 0\n"
    "v_mov_b32 v68, 0\n"
    "v_mov_b32 v69, 0\n"
    "v_mov_b32 v70, 0\n"
    "v_mov_b32 v71, 0\n"
    "v_add_u32 v20, s54, %[l16]\n"
    "v_add_u32 v24, s55, %[l16]\n"
    "v_add_u32 v28, s56, %[l16]\n"
    "v_add_u32 v32, s57, %[l16]\n"
    "v_add_u32 v36, s58, %[l16]\n"
    "s_add_u32 s65, s59, s45\n"
    "s_add_u32 s66, s59, s46\n"
    "s_waitcnt vmcnt(0)\n"
    "s_cmp_lt_u32 s70, s98\n"
    "s_cbranch_scc1 uc0_skip_%=\n"
    "s_lshr_b32 s79, s70, 2\n"
    "s_add_u32 s79, s79, 1\n"
    "s_mov_b32 s74, 0\n"
    "uc0_p_%=:\n"
    "buffer_load_dwordx4 v[80:83], %[ufo], s[40:43], s97 offen sc1\n"
    "s_waitcnt vmcnt(0)\n"
    "v_min3_u32 v80, v80, v81, v82\n"
    "v_min_u32 v80, v80, v83\n"
    "v_cmp_gt_u32 vcc, s79, v80\n"
    "s_cbranch_vccz uc0_got_%=\n"
    "s_add_u32 s74, s74, 1\n"
    "s_sleep 4\n"
    "s_cmp_lt_u32 s74, s76\n"
    "s_cbranch_scc1 uc0_p_%=\n"
    "s_mov_b32 s76, 1\n"
    "uc0_got_%=:\n"
    "s_lshl_b32 s98, s79, 2\n"
    "v_cmp_gt_u32 vcc, 16, v80\n"
    "s_cbranch_vccnz uc0_skip_%=\n"
    "s_mov_b32 s98, 64\n"
    "uc0_skip_%=:\n"
    "s_mov_b32 s30, 0xc8\n"
    "s_mov_b32 s31, 0x258\n"
    "s_mov_b32 s36, 0\n"
    "s_mov_b32 s37, 0x898\n"
    "s_mov_b32 s38, 0x258\n"
    "s_mov_b32 s39, 0\n"
    "s_memtime s[34:35]\n"
    "s_waitcnt lgkmcnt(0)\n"
    "s_mov_b32 s28, s34\n"
    "va_loop_%=:\n"
    "vam_tw_%=:\n"
    "s_memtime s[34:35]\n"
    "s_waitcnt lgkmcnt(0)\n"
    "s_sub_u32 s73, s34, s28\n"
    "s_cmp_lt_i32 s73, 0xffffffec\n"
    "s_cbranch_scc1 vam_tw_%=\n"
    "s_mov_b32 s29, s34\n"
    "v_mov_b32 v84, -1\n"
    "v_mov_b32 v85, -1\n"
    "v_mov_b32 v86, -1\n"
    "v_mov_b32 v87, -1\n"
    "s_mov_b32 s74, 0\n"
    "vam_p_%=:\n"
    "buffer_load_dwordx4 v[12:15], %[l16], s[40:43], s65 offen sc1\n"
    "buffer_load_dwordx4 v[16:19], %[l16], s[40:43], s66 offen sc1\n"
    "s_waitcnt vmcnt(0)\n"
    "v_max3_u32 v72, v12, v13, v14\n"
    "v_max3_u32 v73, v15, v16, v17\n"
    "v_max3_u32 v72, v72, v18, v19\n"
    "v_max_u32 v72, v72, v73\n"
    "v_cmp_eq_u32 vcc, -1, v72\n"
    "ds_write_b128 %[mw0], v[12:15]\n"
    "ds_write_b128 %[mw1], v[16:19]\n"
    "s_cbranch_vccz vam_got_%=\n"
    "s_add_u32 s74, s74, 1\n"
    "s_cmp_lt_u32 s74, s76\n"
    "s_cbranch_scc1 vam_p_%=\n"
    "s_mov_b32 s76, 1\n"
    "vam_got_%=:\n"
    "s_memtime s[34:35]\n"
    "s_mov_b32 s83, s74\n"
    "v_smfmac_f32_16x16x64_bf16 v[40:43], v[12:15], a[0:7], %[idx]\n"
    "v_smfmac_f32_16x16x64_bf16 v[44:47], v[12:15], a[8:15], %[idx]\n"
    "v_smfmac_f32_16x16x64_bf16 v[48:51], v[12:15], a[16:23], %[idx]\n"
    "v_smfmac_f32_16x16x64_bf16 v[52:55], v[12:15], a[24:31], %[idx]\n"
    "v_smfmac_f32_16x16x64_bf16 v[56:59], v[12:15], a[32:39], %[idx]\n"
    "v_smfmac_f32_16x16x64_bf16 v[60:63], v[12:15], a[40:47], %[idx]\n"
    "v_smfmac_f32_16x16x64_bf16 v[64:67], v[12:15], a[48:55], %[idx]\n"
    "v_smfmac_f32_16x16x64_bf16 v[68:71], v[12:15], a[56:63], %[idx]\n"
    "s_waitcnt lgkmcnt(0)\n"
    "s_barrier\n"
    "s_mov_b32 s75, s34\n"
    "s_sub_u32 s36, s34, s29\n"
    "s_min_u32 s36, s36, 0xffff\n"
    "s_lshl_b32 s80, s74, 16\n"
    "s_or_b32 s36, s36, s80\n"
    "buffer_store_dwordx4 v[84:87], %[moff], s[40:43], s61 offen sc1\n"
    "buffer_store_dwordx4 v[76:79], %[moff], s[40:43], s69 offen sc1\n"
    "ds_read_b128 v[36:39], v36\n"
    "ds_read_b128 v[20:23], v20\n"
    "v_smfmac_f32_16x16x64_bf16 v[40:43], v[16:19], a[64:71], %[idx]\n"
    "ds_read_b128 v[24:27], v24\n"
    "ds_read_b128 v[28:31], v28\n"
    "v_smfmac_f32_16x16x64_bf16 v[44:47], v[16:19], a[72:79], %[idx]\n"
    "ds_read_b128 v[32:35], v32\n"
    "v_smfmac_f32_16x16x64_bf16 v[48:51], v[16:19], a[80:87], %[idx]\n"
    "v_smfmac_f32_16x16x64_bf16 v[52:55], v[16:19], a[88:95], %[idx]\n"
    "buffer_store_dwordx4 v[88:91], %[l16], s[40:43], s68 offen sc1\n"
    "v_smfmac_f32_16x16x64_bf16 v[56:59], v[16:19], a[96:103], %[idx]\n"
    "v_smfmac_f32_16x16x64_bf16 v[60:63], v[16:19], a[104:111], %[idx]\n"
    "v_smfmac_f32_16x16x64_bf16 v[64:67], v[16:19], a[112:119], %[idx]\n"
    "buffer_store_dwordx4 v[108:111], %[l16], s[40:43], s68 offen offset:1024 sc1\n"
    "v_smfmac_f32_16x16x64_bf16 v[68:71], v[16:19], a[120:127], %[idx]\n"
    "s_add_u32 s68, s68, 0x1c000\n"
    "s_add_u32 s69, s69, 0x2000\n"
    "ds_read_b128 v[72:75], %[wl]\n"
    "ds_read_b128 v[76:79], %[wl] offset:1024\n"
    "ds_read_b128 v[80:83], %[wl] offset:2048\n"
    "ds_read_b128 v[84:87], %[wl] offset:3072\n"
    "s_waitcnt lgkmcnt(7)\n"
    "v_smfmac_f32_16x16x64_bf16 v[40:43], v[20:23], a[128:135], %[idx]\n"
    "v_smfmac_f32_16x16x64_bf16 v[44:47], v[20:23], a[136:143], %[idx]\n"
    "s_add_u32 s79, s67, 0\n"
    "buffer_load_dwordx4 v[92:95], %[l16], s[40:43], s79 offen sc1\n"
    "v_smfmac_f32_16x16x64_bf16 v[48:51], v[20:23], a[144:151], %[idx]\n"
    "v_smfmac_f32_16x16x64_bf16 v[52:55], v[20:23], a[152:159], %[idx]\n"
    "s_waitcnt lgkmcnt(2)\n"
    "v_smfmac_f32_16x16x64_bf16 v[40:43], v[36:39], v[72:79], %[idx]\n"
    "ds_read_b128 v[72:75], %[wl] offset:4096\n"
    "ds_read_b128 v[76:79], %[wl] offset:5120\n"
    "v_smfmac_f32_16x16x64_bf16 v[56:59], v[20:23], a[160:167], %[idx]\n"
    "v_smfmac_f32_16x16x64_bf16 v[60:63], v[20:23], a[168:175], %[idx]\n"
    "buffer_load_dwordx4 v[112:115], %[l16], s[40:43], s79 offen offset:1024 sc1\n"
    "v_smfmac_f32_16x16x64_bf16 v[64:67], v[20:23], a[176:183], %[idx]\n"
    "v_smfmac_f32_16x16x64_bf16 v[68:71], v[20:23], a[184:191], %[idx]\n"
    "s_waitcnt lgkmcnt(2)\n"
    "v_smfmac_f32_16x16x64_bf16 v[44:47], v[36:39], v[80:87], %[idx]\n"
    "ds_read_b128 v[80:83], %[wl] offset:6144\n"
    "ds_read_b128 v[84:87], %[wl] offset:7168\n"
    "v_smfmac_f32_16x16x64_bf16 v[40:43], v[24:27], a[192:199], %[idx]\n"
    "v_smfmac_f32_16x16x64_bf16 v[44:47], v[24:27], a[200:207], %[idx]\n"
    "s_add_u32 s78, s67, 0x7000\n"
    "buffer_load_dwordx4 v[96:99], %[l16], s[40:43], s78 offen sc1\n"
    "v_smfmac_f32_16x16x64_bf16 v[48:51], v[24:27], a[208:215], %[idx]\n"
    "v_smfmac_f32_16x16x64_bf16 v[52:55], v[24:27], a[216:223], %[idx]\n"
    "s_waitcnt lgkmcnt(2)\n"
    "v_smfmac_f32_16x16x64_bf16 v[48:51], v[36:39], v[72:79], %[idx]\n"
    "ds_read_b128 v[72:75], %[wl] offset:8192\n"
    "ds_read_b128 v[76:79], %[wl] offset:9216\n"
    "v_smfmac_f32_16x16x64_bf16 v[56:59], v[24:27], a[224:231], %[idx]\n"
    "v_smfmac_f32_16x16x64_bf16 v[60:63], v[24:27], a[232:239], %[idx]\n"
    "buffer_load_dwordx4 v[116:119], %[l16], s[40:43], s78 offen offset:1024 sc1\n"
    "v_smfmac_f32_16x16x64_bf16 v[64:67], v[24:27], a[240:247], %[idx]\n"
    "v_smfmac_f32_16x16x64_bf16 v[68:71], v[24:27], a[248:255], %[idx]\n"
    "s_waitcnt lgkmcnt(2)\n"
    "v_smfmac_f32_16x16x64_bf16 v[52:55], v[36:39], v[80:87], %[idx]\n"
    "ds_read_b128 v[80:83], %[wl] offset:10240\n"
    "ds_read_b128 v[84:87], %[wl] offset:11264\n"
    "v_smfmac_f32_16x16x64_bf16 v[40:43], v[28:31], v[128:135], %[idx]\n"
    "v_smfmac_f32_16x16x64_bf16 v[44:47], v[28:31], v[136:143], %[idx]\n"
    "s_add_u32 s79, s67, 0xe000\n"
    "buffer_load_dwordx4 v[100:103], %[l16], s[40:43], s79 offen sc1\n"
    "v_smfmac_f32_16x16x64_bf16 v[48:51], v[28:31], v[144:151], %[idx]\n"
    "v_smfmac_f32_16x16x64_bf16 v[52:55], v[28:31], v[152:159], %[idx]\n"
    "s_waitcnt lgkmcnt(2)\n"
    "v_smfmac_f32_16x16x64_bf16 v[56:59], v[36:39], v[72:79], %[idx]\n"
    "ds_read_b128 v[72:75], %[wl] offset:12288\n"
    "ds_read_b128 v[76:79], %[wl] offset:13312\n"
    "v_smfmac_f32_16x16x64_bf16 v[56:59], v[28:31], v[160:167], %[idx]\n"
    "v_smfmac_f32_16x16x64_bf16 v[60:63], v[28:31], v[168:175], %[idx]\n"
    "buffer_load_dwordx4 v[120:123], %[l16], s[40:43], s79 offen offset:1024 sc1\n"
    "v_smfmac_f32_16x16x64_bf16 v[64:67], v[28:31], v[176:183], %[idx]\n"
    "v_smfmac_f32_16x16x64_bf16 v[68:71], v[28:31], v[184:191], %[idx]\n"
    "s_waitcnt lgkmcnt(2)\n"
    "v_smfmac_f32_16x16x64_bf16 v[60:63], v[36:39], v[80:87], %[idx]\n"
    "ds_read_b128 v[80:83], %[wl] offset:14336\n"
    "ds_read_b128 v[84:87], %[wl] offset:15360\n"
    "v_smfmac_f32_16x16x64_bf16 v[40:43], v[32:35], v[192:199], %[idx]\n"
    "v_smfmac_f32_16x16x64_bf16 v[44:47], v[32:35], v[200:207], %[idx]\n"
    "s_add_u32 s78, s67, 0x15000\n"
    "buffer_load_dwordx4 v[104:107], %[l16], s[40:43], s78 offen sc1\n"
    "v_smfmac_f32_16x16x64_bf16 v[48:51], v[32:35], v[208:215], %[idx]\n"
    "v_smfmac_f32_16x16x64_bf16 v[52:55], v[32:35], v[216:223], %[idx]\n"
    "s_waitcnt lgkmcnt(2)\n"
    "v_smfmac_f32_16x16x64_bf16 v[64:67], v[36:39], v[72:79], %[idx]\n"
    "v_smfmac_f32_16x16x64_bf16 v[56:59], v[32:35], v[224:231], %[idx]\n"
    "v_smfmac_f32_16x16x64_bf16 v[60:63], v[32:35], v[232:239], %[idx]\n"
    "buffer_load_dwordx4 v[124:127], %[l16], s[40:43], s78 offen offset:1024 sc1\n"
    "s_add_u32 s67, s67, 0x70000\n"
    "v_smfmac_f32_16x16x64_bf16 v[64:67], v[32:35], v[240:247], %[idx]\n"
    "v_smfmac_f32_16x16x64_bf16 v[68:71], v[32:35], v[248:255], %[idx]\n"
    "s_waitcnt lgkmcnt(0)\n"
    "v_smfmac_f32_16x16x64_bf16 v[68:71], v[36:39], v[80:87], %[idx]\n"
    "s_nop 7\n"
    "v_permlane32_swap_b32 v40, v56\n"
    "v_permlane32_swap_b32 v41, v57\n"
    "v_permlane32_swap_b32 v42, v58\n"
    "v_permlane32_swap_b32 v43, v59\n"
    "v_permlane32_swap_b32 v48, v64\n"
    "v_permlane32_swap_b32 v49, v65\n"
    "v_permlane32_swap_b32 v50, v66\n"
    "v_permlane32_swap_b32 v51, v67\n"
    "v_add_f32 v40, v40, v56\n"
    "v_add_f32 v41, v41, v57\n"
    "v_add_f32 v42, v42, v58\n"
    "v_add_f32 v43, v43, v59\n"
    "v_add_f32 v48, v48, v64\n"
    "v_add_f32 v49, v49, v65\n"
    "v_add_f32 v50, v50, v66\n"
    "v_add_f32 v51, v51, v67\n"
    "v_cvt_pk_bf16_f32 v88, v40, v41\n"
    "v_cvt_pk_bf16_f32 v89, v48, v49\n"
    "v_cvt_pk_bf16_f32 v90, v42, v43\n"
    "v_cvt_pk_bf16_f32 v91, v50, v51\n"
    "v_permlane32_swap_b32 v44, v60\n"
    "v_permlane32_swap_b32 v45, v61\n"
    "v_permlane32_swap_b32 v46, v62\n"
    "v_permlane32_swap_b32 v47, v63\n"
    "v_permlane32_swap_b32 v52, v68\n"
    "v_permlane32_swap_b32 v53, v69\n"
    "v_permlane32_swap_b32 v54, v70\n"
    "v_permlane32_swap_b32 v55, v71\n"
    "v_add_f32 v44, v44, v60\n"
    "v_add_f32 v45, v45, v61\n"
    "v_add_f32 v46, v46, v62\n"
    "v_add_f32 v47, v47, v63\n"
    "v_add_f32 v52, v52, v68\n"
    "v_add_f32 v53, v53, v69\n"
    "v_add_f32 v54, v54, v70\n"
    "v_add_f32 v55, v55, v71\n"
    "v_cvt_pk_bf16_f32 v108, v44, v45\n"
    "v_cvt_pk_bf16_f32 v109, v52, v53\n"
    "v_cvt_pk_bf16_f32 v110, v46, v47\n"
    "v_cvt_pk_bf16_f32 v111, v54, v55\n"
    "s_cmp_eq_u32 s70, 63\n"
    "s_cbranch_scc1 va_last_%=\n"
    "v_mov_b32 v40, 0\n"
    "v_mov_b32 v41, 0\n"
    "v_mov_b32 v42, 0\n"
    "v_mov_b32 v43, 0\n"
    "v_mov_b32 v44, 0\n"
    "v_mov_b32 v45, 0\n"
    "v_mov_b32 v46, 0\n"
    "v_mov_b32 v47, 0\n"
    "v_mov_b32 v48, 0\n"
    "v_mov_b32 v49, 0\n"
    "v_mov_b32 v50, 0\n"
    "v_mov_b32 v51, 0\n"
    "v_mov_b32 v52, 0\n"
    "v_mov_b32 v53, 0\n"
    "v_mov_b32 v54, 0\n"
    "v_mov_b32 v55, 0\n"
    "v_mov_b32 v56, 0\n"
    "v_mov_b32 v57, 0\n"
    "v_mov_b32 v58, 0\n"
    "v_mov_b32 v59, 0\n"
    "v_mov_b32 v60, 0\n"
    "v_mov_b32 v61, 0\n"
    "v_mov_b32 v62, 0\n"
    "v_mov_b32 v63, 0\n"
    "v_mov_b32 v64, 0\n"
    "v_mov_b32 v65, 0\n"
    "v_mov_b32 v66, 0\n"
    "v_mov_b32 v67, 0\n"
    "v_mov_b32 v68, 0\n"
    "v_mov_b32 v69, 0\n"
    "v_mov_b32 v70, 0\n"
    "v_mov_b32 v71, 0\n"
    "s_add_u32 s78, s70, 1\n"
    "s_cmp_lt_u32 s78, s98\n"
    "s_cbranch_scc1 uc1_skip_%=\n"
    "s_lshr_b32 s79, s78, 2\n"
    "s_add_u32 s79, s79, 1\n"
    "s_mov_b32 s74, 0\n"
    "uc1_p_%=:\n"
    "buffer_load_dwordx4 v[80:83], %[ufo], s[40:43], s97 offen sc1\n"
    "s_waitcnt vmcnt(0)\n"
    "v_min3_u32 v80, v80, v81, v82\n"
    "v_min_u32 v80, v80, v83\n"
    "v_cmp_gt_u32 vcc, s79, v80\n"
    "s_cbranch_vccz uc1_got_%=\n"
    "s_add_u32 s74, s74, 1\n"
    "s_sleep 4\n"
    "s_cmp_lt_u32 s74, s76\n"
    "s_cbranch_scc1 uc1_p_%=\n"
    "s_mov_b32 s76, 1\n"
    "uc1_got_%=:\n"
    "s_lshl_b32 s98, s79, 2\n"
    "v_cmp_gt_u32 vcc, 16, v80\n"
    "s_cbranch_vccnz uc1_skip_%=\n"
    "s_mov_b32 s98, 64\n"
    "uc1_skip_%=:\n"
    "v_mov_b32 v32, 0\n"
    "v_mov_b32 v33, 0\n"
    "v_mov_b32 v34, 0\n"
    "v_mov_b32 v35, 0\n"
    "v_mov_b32 v36, 0\n"
    "v_mov_b32 v37, 0\n"
    "v_mov_b32 v38, 0\n"
    "v_mov_b32 v39, 0\n"
    "s_add_u32 s28, s75, s37\n"
    "vap_tw_%=:\n"
    "s_memtime s[34:35]\n"
    "s_waitcnt lgkmcnt(0)\n"
    "s_sub_u32 s73, s34, s28\n"
    "s_cmp_lt_i32 s73, 0xffffffec\n"
    "s_cbranch_scc1 vap_tw_%=\n"
    "s_mov_b32 s29, s34\n"
    "s_mov_b32 s74, 0\n"
    "vap_p_%=:\n"
    "buffer_load_dwordx4 v[20:23], %[l16], s[40:43], s62 offen sc1\n"
    "buffer_load_dwordx4 v[24:27], %[l16], s[40:43], s62 offen offset:1024 sc1\n"
    "buffer_load_dwordx4 v[28:31], %[l16m], s[40:43], s62 offen offset:2048 sc1\n"
    "s_waitcnt vmcnt(0)\n"
    "v_max3_u32 v72, v20, v21, v22\n"
    "v_max3_u32 v73, v23, v24, v25\n"
    "v_max3_u32 v74, v26, v27, v28\n"
    "v_max3_u32 v75, v29, v30, v31\n"
    "v_max3_u32 v72, v72, v73, v74\n"
    "v_max_u32 v72, v72, v75\n"
    "v_cmp_eq_u32 vcc, -1, v72\n"
    "s_cbranch_vccz vap_got_%=\n"
    "s_add_u32 s74, s74, 1\n"
    "s_cmp_lt_u32 s74, s76\n"
    "s_cbranch_scc1 vap_p_%=\n"
    "s_mov_b32 s76, 1\n"
    "vap_got_%=:\n"
    "s_memtime s[34:35]\n"
    "v_smfmac_f32_16x16x64_bf16 v[32:35], v[20:23], v[88:95], %[idx]\n"
    "v_smfmac_f32_16x16x64_bf16 v[36:39], v[20:23], v[108:115], %[idx]\n"
    "v_smfmac_f32_16x16x64_bf16 v[32:35], v[24:27], v[96:103], %[idx]\n"
    "v_smfmac_f32_16x16x64_bf16 v[36:39], v[24:27], v[116:123], %[idx]\n"
    "v_smfmac_f32_16x16x64_bf16 v[32:35], v[28:31], v[104:111], %[idx]\n"
    "v_smfmac_f32_16x16x64_bf16 v[36:39], v[28:31], v[124:131], %[idx]\n"
    "s_nop 7\n"
    "v_permlane32_swap_b32 v32, v36\n"
    "v_permlane32_swap_b32 v33, v37\n"
    "v_permlane32_swap_b32 v34, v38\n"
    "v_permlane32_swap_b32 v35, v39\n"
    "s_nop 0\n"
    "v_add_f32 v32, v32, v36\n"
    "v_add_f32 v33, v33, v37\n"
    "v_add_f32 v34, v34, v38\n"
    "v_add_f32 v35, v35, v39\n"
    "ds_write_b128 %[redw], v[32:35]\n"
    "s_waitcnt lgkmcnt(0)\n"
    "s_barrier\n"
    "s_sub_u32 s39, s34, s29\n"
    "ds_read2st64_b32 v[72:73], %[redr] offset0:0 offset1:4\n"
    "ds_read2st64_b32 v[74:75], %[redr] offset0:8 offset1:12\n"
    "s_waitcnt lgkmcnt(1)\n"
    "v_add_f32 v72, v72, v73\n"
    "s_waitcnt lgkmcnt(0)\n"
    "v_add_f32 v74, v74, v75\n"
    "v_add_f32 v72, v72, v74\n"
    "s_nop 1\n"
    "v_mov_b32_dpp v73, v72 quad_perm:[1,0,3,2] row_mask:0xf bank_mask:0xf\n"
    "v_cvt_pk_bf16_f32 v76, v72, v73\n"
    "s_nop 1\n"
    "v_mov_b32_dpp v77, v76 row_shl:2 row_mask:0xf bank_mask:0xf\n"
    "v_mov_b32_dpp v78, v76 row_shl:4 row_mask:0xf bank_mask:0xf\n"
    "v_mov_b32_dpp v79, v76 row_shl:6 row_mask:0xf bank_mask:0xf\n"
    "buffer_store_dwordx4 v[76:79], %[moff], s[40:43], s60 offen sc1\n"
    "s_cmp_eq_u32 s70, 62\n"
    "s_cbranch_scc0 va_noemh_%=\n"
    "buffer_store_dwordx4 v[76:79], %[moff], s[40:43], s69 offen sc1\n"
    "va_noemh_%=:\n"
    "s_memtime s[34:35]\n"
    "s_min_u32 s39, s39, 0xffff\n"
    "s_lshl_b32 s80, s74, 16\n"
    "s_or_b32 s39, s39, s80\n"
    "s_cmp_eq_u32 s70, 0\n"
    "s_cbranch_scc1 va_noupd_%=\n"
    "s_and_b32 s80, s36, 0xffff\n"
    "s_sub_u32 s80, s80, s31\n"
    "s_ashr_i32 s81, s80, 1\n"
    "s_min_i32 s81, s81, 0x80\n"
    "s_max_i32 s81, s81, 0xffffff80\n"
    "s_mov_b32 s71, -10\n"
    "s_cmp_gt_i32 s80, 50\n"
    "s_cselect_b32 s71, s81, s71\n"
    "s_cmp_lt_i32 s80, 0xffffffd8\n"
    "s_cselect_b32 s71, s81, s71\n"
    "s_cmp_gt_u32 s36, 0xffff\n"
    "s_cselect_b32 s71, 0x80, s71\n"
    "s_add_u32 s30, s30, s71\n"
    "s_max_i32 s30, s30, 0\n"
    "s_min_i32 s30, s30, 0x3e8\n"
    "s_and_b32 s80, s39, 0xffff\n"
    "s_sub_u32 s80, s80, s38\n"
    "s_ashr_i32 s81, s80, 1\n"
    "s_min_i32 s81, s81, 0x80\n"
    "s_max_i32 s81, s81, 0xffffff80\n"
    "s_mov_b32 s71, -10\n"
    "s_cmp_gt_i32 s80, 50\n"
    "s_cselect_b32 s71, s81, s71\n"
    "s_cmp_lt_i32 s80, 0xffffffd8\n"
    "s_cselect_b32 s71, s81, s71\n"
    "s_cmp_gt_u32 s39, 0xffff\n"
    "s_cselect_b32 s71, 0x80, s71\n"
    "s_add_u32 s37, s37, s71\n"
    "s_max_i32 s37, s37, 0x578\n"
    "s_min_i32 s37, s37, 0xbb8\n"
    "va_noupd_%=:\n"
    "s_waitcnt lgkmcnt(0)\n"
    "s_add_u32 s28, s34, s30\n"
    "s_mov_b32 s73, s59\n"
    "s_mov_b32 s59, s60\n"
    "s_mov_b32 s60, s61\n"
    "s_mov_b32 s61, s73\n"
    "s_mov_b32 s73, s62\n"
    "s_mov_b32 s62, s63\n"
    "s_mov_b32 s63, s64\n"
    "s_mov_b32 s64, s73\n"
    "v_add_u32 v20, s54, %[l16]\n"
    "v_add_u32 v24, s55, %[l16]\n"
    "v_add_u32 v28, s56, %[l16]\n"
    "v_add_u32 v32, s57, %[l16]\n"
    "v_add_u32 v36, s58, %[l16]\n"
    "s_add_u32 s65, s59, s45\n"
    "s_add_u32 s66, s59, s46\n"
    "s_add_u32 s70, s70, 1\n"
    "s_branch va_loop_%=\n"
    "va_last_%=:\n"
    "buffer_store_dwordx4 v[88:91], %[l16], s[40:43], s68 offen sc1\n"
    "buffer_store_dwordx4 v[108:111], %[l16], s[40:43], s68 offen offset:1024 sc1\n"
    "s_waitcnt vmcnt(0)\n"
    "v_mov_b32 v72, 0\n"
    "v_mov_b32 v73, 0\n"
    "v_mov_b32 v74, 0\n"
    "v_mov_b32 v75, 0\n"
    "buffer_store_dwordx4 v[72:75], %[moff], s[40:43], s69 offen sc1\n"
    "s_waitcnt vmcnt(0) lgkmcnt(0)\n"
        :
        : [wslo] "s"(wslo), [wshi] "s"(wshi), [wsend] "s"(WS_SIZE), [wbase] "s"(BIMG_OFF + ((4 * w) * 28 + 2 * v) * 14 * 1024), [kbpack] "s"(pk),
          [ldsbase] "s"(lds), [pbase] "s"((3 + (w >> 1)) * 32768 + (w & 1) * 2560),
          [dbg] "s"(lds + 81920 + w * 4096), [ubase] "s"(UIMG_OFF + ((4 * w) * 28 + 2 * v) * 1024),
          [vmbase] "s"(VM_OFF - 114688 + (w * 28 + 2 * v) * 1024), [mhbase] "s"(MH_OFF), [uflag] "s"(UPROG_OFF),
          [l16] "v"(lane * 16), [idx] "v"((lane & 8) ? 0xEEEE : 0x4444), [mw0] "v"(lds + (pk & 15) * 1024 + lane * 16),
          [mw1] "v"(lds + ((pk >> 4) & 15) * 1024 + lane * 16), [l16m] "v"(lane < 32 ? lane * 16 : 0x7FFFFFF0),
          [wl] "v"(lds + 16384 + w * 16384 + lane * 16),
          [redw] "v"(red + w * 1024 + (g & 1) * 512 + (16 * (lane >> 5) + r16) * 16),
          [redr] "v"(red + (mi >> 2) * 512 + (8 * w + mc) * 16 + (mi & 3) * 4),
          [moff] "v"(mc ? 0x7FFFFFF0 : (4 * v + w) * 128 + mi * 16),
           [ufo] "v"(lane < 28 ? lane * 16 : 0)
        : "v12", "v13", "v14", "v15", "v16", "v17", "v18", "v19", "v20", "v21", "v22", "v23", "v24", "v25", "v26", "v27", "v28", "v29", "v30", "v31",
          "v32", "v33", "v34", "v35", "v36", "v37", "v38", "v39", "v40", "v41", "v42", "v43", "v44", "v45", "v46", "v47", "v48", "v49", "v50", "v51",
          "v52", "v53", "v54", "v55", "v56", "v57", "v58", "v59", "v60", "v61", "v62", "v63", "v64", "v65", "v66", "v67", "v68", "v69", "v70", "v71",
          "v72", "v73", "v74", "v75", "v76", "v77", "v78", "v79", "v80", "v81", "v82", "v83", "v84", "v85", "v86", "v87", "v88", "v89", "v90", "v91",
          "v92", "v93", "v94", "v95", "v96", "v97", "v98", "v99", "v100", "v101", "v102", "v103", "v104", "v105", "v106", "v107", "v108", "v109",
          "v110", "v111", "v112", "v113", "v114", "v115", "v116", "v117", "v118", "v119", "v120", "v121", "v122", "v123", "v124", "v125", "v126",
          "v127", "v128", "v129", "v130", "v131", "v132", "v133", "v134", "v135", "v136", "v137", "v138", "v139", "v140", "v141", "v142", "v143",
          "v144", "v145", "v146", "v147", "v148", "v149", "v150", "v151", "v152", "v153", "v154", "v155", "v156", "v157", "v158", "v159", "v160",
          "v161", "v162", "v163", "v164", "v165", "v166", "v167", "v168", "v169", "v170", "v171", "v172", "v173", "v174", "v175", "v176", "v177",
          "v178", "v179", "v180", "v181", "v182", "v183", "v184", "v185", "v186", "v187", "v188", "v189", "v190", "v191", "v192", "v193", "v194",
          "v195", "v196", "v197", "v198", "v199", "v200", "v201", "v202", "v203", "v204", "v205", "v206", "v207", "v208", "v209", "v210", "v211",
          "v212", "v213", "v214", "v215", "v216", "v217", "v218", "v219", "v220", "v221", "v222", "v223", "v224", "v225", "v226", "v227", "v228",
          "v229", "v230", "v231", "v232", "v233", "v234", "v235", "v236", "v237", "v238", "v239", "v240", "v241", "v242", "v243", "v244", "v245",
          "v246", "v247", "v248", "v249", "v250", "v251", "v252", "v253", "v254", "v255", "a0", "a1", "a2", "a3", "a4", "a5", "a6", "a7", "a8", "a9",
          "a10", "a11", "a12", "a13", "a14", "a15", "a16", "a17", "a18", "a19", "a20", "a21", "a22", "a23", "a24", "a25", "a26", "a27", "a28", "a29",
          "a30", "a31", "a32", "a33", "a34", "a35", "a36", "a37", "a38", "a39", "a40", "a41", "a42", "a43", "a44", "a45", "a46", "a47", "a48", "a49",
          "a50", "a51", "a52", "a53", "a54", "a55", "a56", "a57", "a58", "a59", "a60", "a61", "a62", "a63", "a64", "a65", "a66", "a67", "a68", "a69",
          "a70", "a71", "a72", "a73", "a74", "a75", "a76", "a77", "a78", "a79", "a80", "a81", "a82", "a83", "a84", "a85", "a86", "a87", "a88", "a89",
          "a90", "a91", "a92", "a93", "a94", "a95", "a96", "a97", "a98", "a99", "a100", "a101", "a102", "a103", "a104", "a105", "a106", "a107", "a108",
          "a109", "a110", "a111", "a112", "a113", "a114", "a115", "a116", "a117", "a118", "a119", "a120", "a121", "a122", "a123", "a124", "a125",
          "a126", "a127", "a128", "a129", "a130", "a131", "a132", "a133", "a134", "a135", "a136", "a137", "a138", "a139", "a140", "a141", "a142",
          "a143", "a144", "a145", "a146", "a147", "a148", "a149", "a150", "a151", "a152", "a153", "a154", "a155", "a156", "a157", "a158", "a159",
          "a160", "a161", "a162", "a163", "a164", "a165", "a166", "a167", "a168", "a169", "a170", "a171", "a172", "a173", "a174", "a175", "a176",
          "a177", "a178", "a179", "a180", "a181", "a182", "a183", "a184", "a185", "a186", "a187", "a188", "a189", "a190", "a191", "a192", "a193",
          "a194", "a195", "a196", "a197", "a198", "a199", "a200", "a201", "a202", "a203", "a204", "a205", "a206", "a207", "a208", "a209", "a210",
          "a211", "a212", "a213", "a214", "a215", "a216", "a217", "a218", "a219", "a220", "a221", "a222", "a223", "a224", "a225", "a226", "a227",
          "a228", "a229", "a230", "a231", "a232", "a233", "a234", "a235", "a236", "a237", "a238", "a239", "a240", "a241", "a242", "a243", "a244",
          "a245", "a246", "a247", "a248", "a249", "a250", "a251", "a252", "a253", "a254", "a255", "s28", "s29", "s30", "s31", "s34", "s35", "s36",
          "s37", "s38", "s39", "s40", "s41", "s42", "s43", "s44", "s45", "s46", "s47", "s48", "s49", "s50", "s51", "s52", "s53", "s54", "s55", "s56",
          "s57", "s58", "s59", "s60", "s61", "s62", "s63", "s64", "s65", "s66", "s67", "s68", "s69", "s70", "s71", "s72", "s73", "s74", "s75", "s76",
          "s77", "s78", "s79", "s80", "s81", "s82", "s83", "s84", "s85", "s86", "s87", "s88", "s89", "s90", "s91", "s92", "s93", "s94", "s95", "s96",
          "s97", "s98", "s99", "vcc", "scc", "memory");
}

template <int MASK> __global__ __launch_bounds__(256, 1) void k_main(Params p) {
    __shared__ __attribute__((aligned(16))) char smem[4 * 28672 + 32 * 912];
    const int w = __builtin_amdgcn_readfirstlane(threadIdx.x >> 6);
    rsrc_t ws = mkrsrc(p.ws, WS_SIZE);
    const int x = blockIdx.x & 7, c = blockIdx.x >> 3;
    { const int lane = lane_id(); BSTAMP(0); }
    if (x == 0 || x == 2) {
        if (!(MASK & 2)) return;
        const int ci = x == 0 ? c : 16 + c;
        if (x == 0 && c < 16) score_cu(p, c, w, lane_id(), smem);
        else if (x == 2 && c < 14) value_cu(p, ws, c, w, lane_id(), smem);
        else {
            const int tm = x == 0 ? c - 16 : c - 14;
            if (0 && tm < 14) time_ring(ws, (x >> 1) * 32 + tm, w, lane_id());
            if (x == 0 && c == 30) calibrate(ws, w, lane_id());
            if (0 && x == 2 && c < 30) u_prefetch(ws, (c - 14) * 4 + w, lane_id());
            return;
        }
        BSTAMP(15);
        return;
    }
    const int xi = x == 1 ? 0 : x - 2;
    const bool isu = c < 19;
    const int u = xi * 19 + c, f = (c - 19) * 6 + xi;
    if (isu ? u >= 112 : f >= 64) return;
    int budget = 1 << 17;
    if (isu) {
        if (MASK & 1) u_role(p, ws, u, w, lane_id(), smem);
        if (MASK & 4) {
            const int h = u / 7, j = u % 7;
            for (int G = j, n = 0; G < 16; G += 7, n++) xa_item(p, ws, G, h, w, lane_id(), smem, budget, 3 + 2 * n);
        }
    } else {
        if (MASK & 1) { const int lane = lane_id(); ffn_item(p, ws, f, w, lane, smem); BSTAMP(2); }
        if (MASK & 8)
        {
            for (int k = 0; k < 4; k++)
                if (!2 || k < 3 || f < 56) xb_item<7>(p, ws, (f >> 2) + 16 * k, 7 * (f & 3), w, lane_id(), smem, budget, 3 + k);
            if (2 && (f < 48 || f >= 56)) {
                constexpr int NT = 2 ? 2 : 1, NI = 28 / NT;
                const int idx = f < 48 ? f : f - 8;
                if (idx < 2 * NI) xb_item<NT>(p, ws, NSEG - 2 + idx / NI, idx % NI * NT, w, lane_id(), smem, budget, 7);
            }
        }
    }
}

extern "C" void kernel_launch(void* const* d_in, const int* in_sizes, int n_in, void* d_out, int out_size, void* d_ws, size_t ws_size,
                              hipStream_t stream) {
    Params p;
    p.x = (const u16*)d_in[0]; p.mem0 = (const u16*)d_in[1]; p.Wq = (const u16*)d_in[2]; p.Wk = (const u16*)d_in[3];
    p.Wv = (const u16*)d_in[4]; p.Wo = (const u16*)d_in[5]; p.w1 = (const u16*)d_in[8]; p.w2 = (const u16*)d_in[9];
    p.out = (u16*)d_out;
    p.ws = (char*)(((uintptr_t)d_ws + 524287) & ~(uintptr_t)524287);
    k_prep<<<256, 512, 0, stream>>>(p);
    k_main<15><<<256, 256, 0, stream>>>(p);
}
```
